```python
import torch

def _softmax(x, axis=-1):
    return torch.softmax(x, dim=axis)
def _gelu(x):
    return torch.nn.functional.gelu(x, approximate="tanh")
def _silu(x):
    return torch.nn.functional.silu(x)
def _sigmoid(x):
    return torch.sigmoid(x)
def _layernorm(x, axis=-1, eps=1e-5):
    m = x.mean(dim=axis, keepdim=True)
    v = x.var(dim=axis, keepdim=True, unbiased=False)
    return (x - m) / torch.sqrt(v + eps)
def _scatter_add(tgt, idx, src, axis=0):
    return tgt.clone().index_add_(axis, idx, src)
def _segment_sum(src, seg, n):
    out = torch.zeros((n,) + src.shape[1:], dtype=src.dtype)
    return out.index_add_(0, seg, src)
def _pad(x, pw):
    flat = tuple(v for pair in reversed(pw) for v in pair)
    return torch.nn.functional.pad(x, flat)

kernel_name = 'interleave_layers__linear_kernel_hedgehog__mixer_s_mlp__prompt'

def setup_inputs():

    _w = (lambda a: (lambda t: t.to(torch.bfloat16) if t.is_floating_point() else t)(torch.as_tensor(a)))
    def _A_setup():
        B, S, D, H = 1, 2048, 640, 32
        Dh = D // H
        return {
            "x": _w((lambda *s: torch.randn(*s, dtype=torch.bfloat16) * 0.05)(B, S, D)),
            "wqkv": _w((lambda *s: torch.randn(*s, dtype=torch.bfloat16) * 0.4)(D, 3 * D)),
            "w_phi": _w((lambda *s: torch.randn(*s, dtype=torch.bfloat16) * 0.4)(H, Dh, Dh) * (Dh ** -0.5)),
            "wo": _w((lambda *s: torch.randn(*s, dtype=torch.bfloat16) * 0.05)(D, D)),
        }
    _A_in = _A_setup()

    def _B_setup():
        B, S, D, F = 1, 2048, 640, 1536
        return {
            "x":  _w((lambda *s: torch.randn(*s, dtype=torch.bfloat16) * 0.05)(B, S, D)),
            "w1": _w((lambda *s: torch.randn(*s, dtype=torch.bfloat16) * 3.2)(D, D) * (D ** -0.5)),
            "w2": _w((lambda *s: torch.randn(*s, dtype=torch.bfloat16) * 3.2)(D, D) * (D ** -0.5)),
            "w3": _w((lambda *s: torch.randn(*s, dtype=torch.bfloat16) * 3.2)(D, F) * (D ** -0.5)),
            "w4": _w((lambda *s: torch.randn(*s, dtype=torch.bfloat16) * 3.2)(F, D) * (F ** -0.5)),
        }
    _B_in = _B_setup()

    return {
        "A_x": _A_in["x"],
        "A_wqkv": _A_in["wqkv"],
        "A_w_phi": _A_in["w_phi"],
        "A_wo": _A_in["wo"],
        "B_x": _B_in["x"],
        "B_w1": _B_in["w1"],
        "B_w2": _B_in["w2"],
        "B_w3": _B_in["w3"],
        "B_w4": _B_in["w4"],
    }

def reference(A_x, A_wqkv, A_w_phi, A_wo, B_x, B_w1, B_w2, B_w3, B_w4):
    def _A_ref(x, wqkv, w_phi, wo):
        B, S, D = x.shape
        H = 32; Dh = D // H
        qkv = (lambda x, *d: x.permute(*d))((x @ wqkv).reshape(B, S, 3, H, Dh), 2, 0, 3, 1, 4)
        q, k, v = qkv[0], qkv[1], qkv[2]
        pq = torch.einsum("bhsd,hdf->bhsf", q, w_phi)
        pk = torch.einsum("bhsd,hdf->bhsf", k, w_phi)
        phi_q = torch.concatenate([_softmax(pq, axis=-1), _softmax(-pq, axis=-1)], axis=-1)
        phi_k = torch.concatenate([_softmax(pk, axis=-1), _softmax(-pk, axis=-1)], axis=-1)
        kv = torch.einsum("bhsf,bhsd->bhfd", phi_k, v)
        z  = torch.einsum("bhsf->bhf", phi_k)
        num = torch.einsum("bhqf,bhfd->bhqd", phi_q, kv)
        den = torch.einsum("bhqf,bhf->bhq", phi_q, z)[..., None] + 1e-6
        o = num / den
        return (lambda x, *d: x.permute(*d))(o, 0, 2, 1, 3).reshape(B, S, D) @ wo

    def _B_ref(x, w1, w2, w3, w4):
        B, S, D = x.shape
        h = _gelu(_layernorm(x, axis=-1) @ w1)
        c = D // 4
        g0 = _pad(h[:, :, 0*c:1*c], ((0, 0), (1, 0), (0, 0)))[:, :S, :]
        g1 = _pad(h[:, :, 1*c:2*c], ((0, 0), (0, 1), (0, 0)))[:, 1:, :]
        g2 = _pad(h[:, :, 2*c:3*c], ((0, 0), (2, 0), (0, 0)))[:, :S, :]
        g3 = _pad(h[:, :, 3*c:   ], ((0, 0), (0, 2), (0, 0)))[:, 2:, :]
        hs = torch.concatenate([g0, g1, g2, g3], axis=-1) @ w2
        x1 = x + hs
        h2 = _gelu(_layernorm(x1, axis=-1) @ w3) @ w4
        return x1 + h2

    x = A_x
    x = x + _A_ref(_layernorm(x, axis=-1), A_wqkv, A_w_phi, A_wo)
    x = x + _B_ref(_layernorm(x, axis=-1), B_w1, B_w2, B_w3, B_w4)
    x = x + _A_ref(_layernorm(x, axis=-1), A_wqkv, A_w_phi, A_wo)
    x = x + _B_ref(_layernorm(x, axis=-1), B_w1, B_w2, B_w3, B_w4)
    return x
```

```cpp
#include <hip/hip_runtime.h>
#include <hip/hip_bf16.h>
#include <stdint.h>

#define DI __device__ __forceinline__
#define LAS __attribute__((address_space(3)))
#define GAS __attribute__((address_space(1)))
typedef unsigned v4u __attribute__((ext_vector_type(4)));
typedef unsigned v2u __attribute__((ext_vector_type(2)));
typedef float f32x4 __attribute__((ext_vector_type(4)));
typedef float f32x2 __attribute__((ext_vector_type(2)));
typedef short bf16x8 __attribute__((ext_vector_type(8)));
typedef short s16x4 __attribute__((ext_vector_type(4)));
typedef __bf16 bf2_t __attribute__((ext_vector_type(2)));
typedef __amdgpu_buffer_rsrc_t srd_t;
typedef unsigned long long u64;

#define STOP_PHASE 1000
#define STAMPS 0
#define FINE 0
#define LATEW(w) ((w) == 7)
#define FST(id) { if (FINE) stamp(k, sb + (id)); }

constexpr int S_ = 2048, D_ = 640, F_ = 1536;
constexpr unsigned WS_EPOCH = 0, WS_TMO = 64;
constexpr unsigned WS_FLAGS = 4096;
constexpr unsigned WS_CS = 65536;
constexpr unsigned WS_STAMP = 131072;
constexpr unsigned WS_RED = 1u << 20;
constexpr unsigned WS_STAT = WS_RED + (1u << 19);
constexpr unsigned STAT_SZ = 128 * 2048;
constexpr unsigned WS_PART = 4u << 20;
constexpr unsigned PART_L = 256u * 16384u;
constexpr unsigned ACT20 = 128u * 20u * 1024u, ACT48 = 128u * 48u * 1024u;
constexpr unsigned WS_ACT = 16u << 20;
constexpr unsigned ACT_L = 5u * ACT20 + ACT48;
constexpr unsigned A_O = 0, A_X2 = ACT20, A_HS = 2 * ACT20, A_X1 = 3 * ACT20, A_XO = 4 * ACT20, A_H2 = 5 * ACT20;
constexpr unsigned WS_WT = 64u << 20;
constexpr unsigned WT_A = 0, WT_O = 327680, WT_1 = WT_O + 102400, WT_2 = WT_1 + 102400, WT_4 = WT_2 + 102400, WT_3 = WT_4 + 245760,
                   WT_C = WT_3 + 245760;
constexpr unsigned WS_END = WS_WT + 8 * WT_C;
enum { PH_KV1 = 0, PH_KV2, PH_O, PH_X2, PH_H, PH_X1, PH_H2, PH_XO, PH_PER_LAYER, PH_CS = 16, PH_T1 = 17, PH_T2 = 18 };
constexpr unsigned SLOT = 5120, NSLOT = 31, RINGP = 155;
constexpr unsigned L_MISC = SLOT * NSLOT;
constexpr unsigned L_STATS = L_MISC;
constexpr unsigned L_CSR = L_MISC + 512;
constexpr unsigned LDS_BYTES = 163840;
constexpr unsigned L_X = 0, L_XF = 76800;
constexpr unsigned L_IMG = 0, IMG_H = 10240;
constexpr unsigned L_S0 = 81920, L_SJ = 97280, L_SCS = 61440;
constexpr int P_C = 8, P_D = 28, P_E = 17, P_G = 6;
constexpr unsigned OOB = 0x80000000u;

#define WAIT_VM(n) asm volatile("s_waitcnt vmcnt(" #n ")" ::: "memory")
#define BAR() asm volatile("s_barrier" ::: "memory")
#define BAR_L() asm volatile("s_waitcnt lgkmcnt(0)\n\ts_barrier" ::: "memory")
#define BAR_VL0() asm volatile("s_waitcnt vmcnt(0) lgkmcnt(0)\n\ts_barrier" ::: "memory")
#define SB() __builtin_amdgcn_sched_barrier(0)
template <int N> DI void bar_vl() { asm volatile("s_waitcnt vmcnt(%0) lgkmcnt(0)\n\ts_barrier" ::"i"(N) : "memory"); }

DI void glds(unsigned lds, unsigned voff, srd_t srd, unsigned soff) {
  asm volatile("s_mov_b32 m0, %0\n\ts_nop 0\n\tbuffer_load_dwordx4 %1, %2, %3 offen lds" ::"s"(lds), "v"(voff), "s"(srd), "s"(soff) : "memory");
}
template <int N> DI void gln(unsigned lds, unsigned voff, srd_t srd, unsigned soff);
template <> DI void gln<1>(unsigned lds, unsigned voff, srd_t srd, unsigned soff) {
  asm volatile("s_mov_b32 m0, %0\n\ts_nop 0\n\tbuffer_load_dwordx4 %1, %2, %3 offen sc1 lds" ::"s"(lds), "v"(voff), "s"(srd), "s"(soff) : "memory");
}
template <> DI void gln<2>(unsigned lds, unsigned voff, srd_t srd, unsigned soff) {
  asm volatile("s_mov_b32 m0, %0\n\ts_nop 0\n\tbuffer_load_dwordx4 %1, %2, %3 offen sc1 lds\n\t"
               "buffer_load_dwordx4 %1, %2, %3 offen offset:1024 sc1 lds" ::"s"(lds), "v"(voff), "s"(srd), "s"(soff) : "memory");
}
template <> DI void gln<3>(unsigned lds, unsigned voff, srd_t srd, unsigned soff) {
  asm volatile("s_mov_b32 m0, %0\n\ts_nop 0\n\tbuffer_load_dwordx4 %1, %2, %3 offen sc1 lds\n\t"
               "buffer_load_dwordx4 %1, %2, %3 offen offset:1024 sc1 lds\n\tbuffer_load_dwordx4 %1, %2, %3 offen offset:2048 sc1 lds"
               ::"s"(lds), "v"(voff), "s"(srd), "s"(soff) : "memory");
}
template <> DI void gln<4>(unsigned lds, unsigned voff, srd_t srd, unsigned soff) {
  asm volatile("s_mov_b32 m0, %0\n\ts_nop 0\n\tbuffer_load_dwordx4 %1, %2, %3 offen sc1 lds\n\t"
               "buffer_load_dwordx4 %1, %2, %3 offen offset:1024 sc1 lds\n\tbuffer_load_dwordx4 %1, %2, %3 offen offset:2048 sc1 lds\n\t"
               "buffer_load_dwordx4 %1, %2, %3 offen offset:3072 sc1 lds" ::"s"(lds), "v"(voff), "s"(srd), "s"(soff) : "memory");
}
template <> DI void gln<6>(unsigned lds, unsigned voff, srd_t srd, unsigned soff);
template <> DI void gln<5>(unsigned lds, unsigned voff, srd_t srd, unsigned soff) { gln<4>(lds, voff, srd, soff); gln<1>(lds + 4096, voff, srd, soff + 4096); }
template <> DI void gln<6>(unsigned lds, unsigned voff, srd_t srd, unsigned soff) { gln<4>(lds, voff, srd, soff); gln<2>(lds + 4096, voff, srd, soff + 4096); }
template <int P> DI void gl1(unsigned lds, unsigned voff, srd_t srd, unsigned soff) {
  asm volatile("s_mov_b32 m0, %0\n\ts_nop 0\n\tbuffer_load_dwordx4 %1, %2, %3 offen offset:%4 sc1 lds"
               ::"s"(lds + (P >> 2) * 4096u), "v"(voff), "s"(srd), "s"(soff + (P >> 2) * 4096u), "i"((P & 3) * 1024) : "memory");
}
template <int OFF> DI void ld128(v4u& d, unsigned voff, srd_t srd, unsigned soff) {
  asm volatile("buffer_load_dwordx4 %0, %1, %2, %3 offen offset:%4 sc1" : "=v"(d) : "v"(voff), "s"(srd), "s"(soff), "i"(OFF) : "memory");
}
template <int OFF> DI void ld64(v2u& d, unsigned voff, srd_t srd, unsigned soff) {
  asm volatile("buffer_load_dwordx2 %0, %1, %2, %3 offen offset:%4 sc1" : "=v"(d) : "v"(voff), "s"(srd), "s"(soff), "i"(OFF) : "memory");
}
template <int N> DI void wait_tie(v4u& a) { asm volatile("s_waitcnt vmcnt(%1)" : "+v"(a) : "i"(N)); }
template <int N> DI void wait_tie2(v4u& a, v4u& b) { asm volatile("s_waitcnt vmcnt(%2)" : "+v"(a), "+v"(b) : "i"(N)); }
template <int N> DI void wait_tie3(v4u& a, v4u& b, v4u& c) { asm volatile("s_waitcnt vmcnt(%3)" : "+v"(a), "+v"(b), "+v"(c) : "i"(N)); }
DI void tie2(v4u& a, v4u& b) { asm volatile("" : "+v"(a), "+v"(b)); }
DI void tie3(v4u& a, v4u& b, v4u& c) { asm volatile("" : "+v"(a), "+v"(b), "+v"(c)); }
DI void tie1(v4u& a) { asm volatile("" : "+v"(a)); }
DI void st128(v4u v, unsigned voff, srd_t srd, unsigned soff) { __builtin_amdgcn_raw_buffer_store_b128(v, srd, voff, soff, 16); }
DI void st64(v2u v, unsigned voff, srd_t srd, unsigned soff) { __builtin_amdgcn_raw_buffer_store_b64(v, srd, voff, soff, 16); }

DI float asf(unsigned u) { return __builtin_bit_cast(float, u); }
DI unsigned asu(float f) { return __builtin_bit_cast(unsigned, f); }
DI srd_t mk_srd(const void* p, unsigned bytes) { return __builtin_amdgcn_make_buffer_rsrc((void*)p, 0, bytes, 0x00020000); }
DI f32x4 mfma(v4u a, v4u b, f32x4 c) {
  return __builtin_amdgcn_mfma_f32_16x16x32_bf16(__builtin_bit_cast(bf16x8, a), __builtin_bit_cast(bf16x8, b), c, 0, 0, 0);
}
DI unsigned pk2(float a, float b) { f32x2 v = {a, b}; return __builtin_bit_cast(unsigned, __builtin_convertvector(v, bf2_t)); }
DI unsigned pk2v(f32x2 v) { return __builtin_bit_cast(unsigned, __builtin_convertvector(v, bf2_t)); }
DI float bflo(unsigned u) { return asf(u << 16); }
DI float bfhi(unsigned u) { return asf(u & 0xFFFF0000u); }
DI f32x4 unpk(v2u u) { return f32x4{bflo(u.x), bfhi(u.x), bflo(u.y), bfhi(u.y)}; }
DI v2u pk4(f32x4 v) { return v2u{pk2(v.x, v.y), pk2(v.z, v.w)}; }
DI f32x4 rnd4(f32x4 v) { return unpk(pk4(v)); }
template <int ROW8> DI v4u trfrag(LAS unsigned char* p) {
  s16x4 lo = __builtin_amdgcn_ds_read_tr16_b64_v4i16((LAS s16x4*)p);
  s16x4 hi = __builtin_amdgcn_ds_read_tr16_b64_v4i16((LAS s16x4*)(p + ROW8));
  return __builtin_bit_cast(v4u, __builtin_shufflevector(lo, hi, 0, 1, 2, 3, 4, 5, 6, 7));
}
DI int tr_row(int lane) { const int g = lane >> 4; return 16 * (g >> 1) + 4 * (g & 1) + ((lane >> 2) & 3); }
DI void xr_sum2(float& a, float& b) {
  auto s = __builtin_amdgcn_permlane16_swap(asu(a), asu(b), false, false);
  const float m = asf(s[0]) + asf(s[1]);
  auto t = __builtin_amdgcn_permlane32_swap(asu(m), asu(m), false, false);
  const float n = asf(t[0]) + asf(t[1]);
  auto u = __builtin_amdgcn_permlane16_swap(asu(n), asu(n), false, false);
  a = asf(u[0]); b = asf(u[1]);
}
DI float fmax_raw(float a, float b) { float r; asm("v_max_f32 %0, %1, %2" : "=v"(r) : "v"(a), "v"(b)); return r; }
DI float fmax3_raw(float a, float b, float c) { float r; asm("v_max3_f32 %0, %1, %2, %3" : "=v"(r) : "v"(a), "v"(b), "v"(c)); return r; }
DI float fmin3_raw(float a, float b, float c) { float r; asm("v_min3_f32 %0, %1, %2, %3" : "=v"(r) : "v"(a), "v"(b), "v"(c)); return r; }
DI void xr_max2(float& a, float& b) {
  auto s = __builtin_amdgcn_permlane16_swap(asu(a), asu(b), false, false);
  const float m = __builtin_fmaxf(asf(s[0]), asf(s[1]));
  auto t = __builtin_amdgcn_permlane32_swap(asu(m), asu(m), false, false);
  const float n = __builtin_fmaxf(asf(t[0]), asf(t[1]));
  auto u = __builtin_amdgcn_permlane16_swap(asu(n), asu(n), false, false);
  a = asf(u[0]); b = asf(u[1]);
}
template <bool MAX> DI void xr4(float (&a)[4], float (&b)[4]) {
  float m[4], n[4];
#pragma unroll
  for (int i = 0; i < 4; ++i) { auto s = __builtin_amdgcn_permlane16_swap(asu(a[i]), asu(b[i]), false, false);
                                m[i] = MAX ? fmax_raw(asf(s[0]), asf(s[1])) : asf(s[0]) + asf(s[1]); }
#pragma unroll
  for (int i = 0; i < 4; ++i) { auto t = __builtin_amdgcn_permlane32_swap(asu(m[i]), asu(m[i]), false, false);
                                n[i] = MAX ? fmax_raw(asf(t[0]), asf(t[1])) : asf(t[0]) + asf(t[1]); }
#pragma unroll
  for (int i = 0; i < 4; ++i) { auto u = __builtin_amdgcn_permlane16_swap(asu(n[i]), asu(n[i]), false, false); a[i] = asf(u[0]); b[i] = asf(u[1]); }
}
DI f32x2 gelu2(f32x2 x) {
  constexpr float C0 = -2.0f * 0.7978845608028654f * 1.4426950408889634f, C1 = C0 * 0.044715f;
  const f32x2 a = x * ((x * x) * C1 + C0);
  const f32x2 d = f32x2{__builtin_amdgcn_exp2f(a.x), __builtin_amdgcn_exp2f(a.y)} + 1.0f;
  return x * f32x2{__builtin_amdgcn_rcpf(d.x), __builtin_amdgcn_rcpf(d.y)};
}
DI v2u ln_gelu4(f32x4 acc, f32x4 cs, float nmean, float scale) {
  const f32x2 a = (f32x2{cs.x, cs.y} * nmean + f32x2{acc.x, acc.y}) * scale, b = (f32x2{cs.z, cs.w} * nmean + f32x2{acc.z, acc.w}) * scale;
  return v2u{pk2v(gelu2(a)), pk2v(gelu2(b))};
}
DI int opq(int x) { asm volatile("" : "+v"(x)); return x; }
DI int opqs(int x) { asm volatile("" : "+s"(x)); return x; }
template <class T> DI T* opqp(T* p) { asm volatile("" : "+s"(p)); return p; }

struct Ctx {
  unsigned char* ws;
  LAS unsigned char* L;
  int w, r, c, b;
  u64 tag;
  bool dead;
  DI void fresh() { w = opqs(w); r = opqs(r); c = opqs(c); ws = opqp(ws); }
};

DI void stamp(const Ctx&, int) {}
DI void stampc(const Ctx&, int) {}
DI void stamp_dump(const Ctx&) {}
DI void stampw(const Ctx&, int) {}

DI void poll_flags(Ctx& k, unsigned flag_byte_off, u64 want) {
  if (k.w == 7 && !k.dead) {
    GAS u64* p = (GAS u64*)(k.ws + WS_FLAGS + flag_byte_off);
    unsigned spins = 0;
    for (;;) {
      const u64 v = __hip_atomic_load(p, __ATOMIC_RELAXED, __HIP_MEMORY_SCOPE_AGENT);
      if (__all(v == want)) break;
      if (++spins > (1u << 20)) { k.dead = true; __hip_atomic_store((GAS unsigned*)(k.ws + WS_TMO), 1u, __ATOMIC_RELAXED, __HIP_MEMORY_SCOPE_AGENT); break; }
    }
  }
}
DI void poll_flags_any(Ctx& k, unsigned flag_byte_off, u64 want) {
  if (!k.dead) {
    GAS u64* p = (GAS u64*)(k.ws + WS_FLAGS + flag_byte_off);
    unsigned spins = 0;
    for (;;) {
      const u64 v = __hip_atomic_load(p, __ATOMIC_RELAXED, __HIP_MEMORY_SCOPE_AGENT);
      if (__all(v == want)) break;
      if (++spins > (1u << 20)) { k.dead = true; __hip_atomic_store((GAS unsigned*)(k.ws + WS_TMO), 1u, __ATOMIC_RELAXED, __HIP_MEMORY_SCOPE_AGENT); break; }
    }
  }
}
DI v4u granule(f32x4 v, u64 tg) { return v4u{pk2(v.x, v.y), (unsigned)tg, pk2(v.z, v.w), (unsigned)(tg >> 32)}; }
DI bool granule_ok(v4u g, u64 tg) { return g.y == (unsigned)tg && g.w == (unsigned)(tg >> 32); }
DI bool spin_more(Ctx& k, unsigned& spins) {
  if (k.dead) return false;
  __builtin_amdgcn_s_sleep(2);
  if (++spins > (1u << 19)) { k.dead = true; __hip_atomic_store((GAS unsigned*)(k.ws + WS_TMO), 1u, __ATOMIC_RELAXED, __HIP_MEMORY_SCOPE_AGENT); return false; }
  return true;
}
DI void post_flag(const Ctx& k, unsigned flag_byte_off, u64 v) {
  if (threadIdx.x == 7 * 64) __hip_atomic_store((GAS u64*)(k.ws + WS_FLAGS + flag_byte_off), v, __ATOMIC_RELAXED, __HIP_MEMORY_SCOPE_AGENT);
}
DI u64 tag_of(const Ctx& k, int ph) { return k.tag + (u64)(ph + 1); }
DI unsigned fl_group(int ph, int r, int c) { return (unsigned)((ph * 256 + r * 8 + c) * 8); }
DI unsigned fl_col(int ph, int r, int c) { return (unsigned)((ph * 256 + c * 32 + r) * 8); }

template <int NW> DI void wstream(const Ctx& k, srd_t ws, unsigned src, int lp, int np, unsigned v16) {
  if (k.w < NW) {
#pragma unroll 1
    for (int g = k.w * 4; g < np; g += NW * 4) {
      const int s = g + 4 > np ? np - 4 : g;
      gln<4>((unsigned)(lp + s) * 1024u, v16, ws, src + (unsigned)s * 1024u);
    }
  }
}
template <int G> DI void wstream_exact(const Ctx& k, srd_t ws, unsigned src, int lp, int np, unsigned v16) {
  const int g = k.w * G, s = g + G > np ? np - G : g;
  gln<G>((unsigned)(lp + s) * 1024u, v16, ws, src + (unsigned)s * 1024u);
}

struct StatLd { v2u s0, s1, s2, s3; };
DI void stat_issue(StatLd& s, srd_t ws, unsigned statbuf, int tb, int lane) {
  const unsigned vo = (unsigned)((lane >> 4) * 512 + (lane & 15) * 8);
  const unsigned so = WS_STAT + statbuf * STAT_SZ + (unsigned)tb * 2048u;
  ld64<0>(s.s0, vo, ws, so); ld64<128>(s.s1, vo, ws, so); ld64<256>(s.s2, vo, ws, so); ld64<384>(s.s3, vo, ws, so);
}
DI void stat_finish(const StatLd& s, float& mean, float& var) {
  float a = (asf(s.s0.x) + asf(s.s1.x)) + (asf(s.s2.x) + asf(s.s3.x));
  float q = (asf(s.s0.y) + asf(s.s1.y)) + (asf(s.s2.y) + asf(s.s3.y));
  xr_sum2(a, q);
  mean = a * (1.0f / 640.0f);
  var = __builtin_fmaxf(__builtin_fmaf(-mean, mean, q * (1.0f / 640.0f)), 0.0f);
}
template <int N> DI void wait_tie_stat(StatLd& s) { asm volatile("s_waitcnt vmcnt(%4)" : "+v"(s.s0), "+v"(s.s1), "+v"(s.s2), "+v"(s.s3) : "i"(N)); }

#define MIX(NM, ND) { _Pragma("unroll") for (int m_ = 0; m_ < ND; ++m_) { __builtin_amdgcn_sched_group_barrier(0x008, NM, 0); __builtin_amdgcn_sched_group_barrier(0x100, 1, 0); } }
struct RingAddr { LAS unsigned char *b0, *b1; };
DI RingAddr ring_addr(const Ctx& k, int lane) {
  const int kh = k.w >> 2;
  return RingAddr{k.L + lane * 16 + kh * SLOT, k.L + lane * 16 + (kh ? 0 : 30 * SLOT)};
}
template <int N0> DI void ring_read(const RingAddr& a, v4u (&d)[5]) {
  constexpr int sA = N0 % 31;
  LAS unsigned char* p = sA == 30 ? a.b1 : a.b0 + sA * SLOT;
#pragma unroll
  for (int bl = 0; bl < 5; ++bl) d[bl] = *(LAS v4u*)(p + 1024 * bl);
}
#define KMAIN(P, NS, NL, PF)                                                                                           \
  {                                                                                                                    \
    v4u wf[2][5];                                                                                                      \
    ring_read<P>(ra, wf[0]);                                                                                           \
    KSTEP(P, NS, NL, PF, 0) KSTEP(P, NS, NL, PF, 1) KSTEP(P, NS, NL, PF, 2) KSTEP(P, NS, NL, PF, 3) KSTEP(P, NS, NL, PF, 4) \
    KSTEP(P, NS, NL, PF, 5) KSTEP(P, NS, NL, PF, 6) KSTEP(P, NS, NL, PF, 7) KSTEP(P, NS, NL, PF, 8) KSTEP(P, NS, NL, PF, 9) \
  }
#define KSTEP(P, NS, NL, PF, i)                                                               \
  {                                                                                           \
    wait_tie<9 - i + NS + (i < 1 ? 0 : (i - 1 < NL ? i - 1 : NL))>(xf[i]);                    \
    if (i + 1 < 10) ring_read<P + 2 * (i + 1)>(ra, wf[(i + 1) & 1]);                          \
    _Pragma("unroll") for (int bl = 0; bl < 5; ++bl) acc[bl] = mfma(wf[i & 1][bl], xf[i], acc[bl]); \
    if (i + 1 < 10) MIX(1, 5)                                                                 \
    SB();                                                                                     \
    if (i >= 1 && i <= NL) PF((i >= 1 ? i - 1 : 0))                                           \
  }
DI unsigned pin(unsigned x) { asm volatile("" : "+s"(x)); return x; }
DI unsigned pinv(unsigned x) { asm volatile("" : "+v"(x)); return x; }
DI unsigned kx_off(const Ctx& k, unsigned actbuf) { return pin(actbuf + (unsigned)k.r * 81920u + (unsigned)(k.w >> 2) * 40960u + (unsigned)(k.w & 3) * 1024u); }
DI void kx_issue(v4u (&x)[10], srd_t ws, unsigned so, unsigned vo) {
#pragma unroll
  for (int i = 0; i < 10; ++i) ld128<0>(x[i], vo, ws, so + i * 4096u);
}
DI void kexchange(const Ctx& k, f32x4 (&acc)[5], f32x4 (&fin)[3], unsigned scratch, int lane) {
  LAS f32x4* sc = (LAS f32x4*)(k.L + scratch + (unsigned)(k.w & 3) * 5120u + lane * 16);
  if ((k.w >> 2) == 0) { sc[3 * 64] = acc[3]; sc[4 * 64] = acc[4]; fin[0] = acc[0]; fin[1] = acc[1]; fin[2] = acc[2]; }
  else { sc[0] = acc[0]; sc[64] = acc[1]; sc[128] = acc[2]; fin[0] = acc[3]; fin[1] = acc[4]; fin[2] = f32x4{0.f, 0.f, 0.f, 0.f}; }
  BAR_L();
  const int fb = (k.w >> 2) * 3;
  fin[0] += sc[fb * 64]; fin[1] += sc[(fb + 1) * 64];
  if (fb == 0) fin[2] += sc[128];
}
DI unsigned frag_off(int B, int gq, int t) { return (unsigned)((B >> 1) * 4096 + (2 * (B & 1) + (gq & 1)) * 256 + t * 16 + (gq >> 1) * 8); }
DI unsigned blk_off(int B) { return (unsigned)((B >> 1) * 4096 + (B & 1) * 512); }
DI void kstore(const Ctx& k, srd_t ws, unsigned actbuf, const v2u (&pk)[3], int lane) {
  const int kh = k.w >> 2;
  const unsigned lo = frag_off(0, lane >> 4, lane & 15);
  const unsigned so = actbuf + (unsigned)k.r * 81920u + (unsigned)(k.w & 3) * 1024u;
#pragma unroll
  for (int j = 0; j < 3; ++j) {
    const int B = 5 * k.c + 3 * kh + j;
    if (!(j == 2 && kh)) st64(pk[j], lo, ws, so + blk_off(B));
  }
}
DI void kstats(const Ctx& k, srd_t ws, unsigned statbuf, const f32x4 (&v)[3], int lane) {
  const int kh = k.w >> 2;
  f32x2 s = f32x2{v[0].x, v[0].y} + f32x2{v[0].z, v[0].w} + f32x2{v[1].x, v[1].y} + f32x2{v[1].z, v[1].w};
  f32x2 q = f32x2{v[0].x, v[0].y} * f32x2{v[0].x, v[0].y} + f32x2{v[0].z, v[0].w} * f32x2{v[0].z, v[0].w};
  q = f32x2{v[1].x, v[1].y} * f32x2{v[1].x, v[1].y} + q; q = f32x2{v[1].z, v[1].w} * f32x2{v[1].z, v[1].w} + q;
  if (kh == 0) {
    s += f32x2{v[2].x, v[2].y} + f32x2{v[2].z, v[2].w};
    q = f32x2{v[2].x, v[2].y} * f32x2{v[2].x, v[2].y} + q; q = f32x2{v[2].z, v[2].w} * f32x2{v[2].z, v[2].w} + q;
  }
  float s1 = s.x + s.y, q1 = q.x + q.y;
  xr_sum2(s1, q1);
  const unsigned vo = lane < 16 ? (unsigned)(lane * 8) : OOB;
  st64(v2u{asu(s1), asu(q1)}, vo, ws, WS_STAT + statbuf * STAT_SZ + (unsigned)((k.r * 4 + (k.w & 3)) * 2048 + (2 * k.c + kh) * 128));
}
constexpr int g_xwait(int s) {
  constexpr int t[24] = {63, 8, 8, 8, 8, 10, 11, 12, 13, 15, 16, 17, 18, 18, 16, 15, 14, 12, 9, 7, 63, 63, 63, 63};
  return t[s];
}

__global__ void __launch_bounds__(512, 2)
hedgehog_smlp_kernel(const unsigned char* x, const unsigned char* wqkv, const unsigned char* wphi, const unsigned char* wo,
                     const unsigned char* w1, const unsigned char* w2, const unsigned char* w3, const unsigned char* w4,
                     unsigned char* out, unsigned char* wsp) {
  extern __shared__ __attribute__((aligned(16))) unsigned char smem[];
  Ctx k;
  k.ws = wsp;
  k.L = (LAS unsigned char*)smem;
  const int tid = threadIdx.x;
  const int lane0 = tid & 63;
  k.w = __builtin_amdgcn_readfirstlane(tid >> 6);
  k.b = blockIdx.x;
#define MAPMODE 2
  { const int xc = k.b & 7, i = k.b >> 3;
    if (MAPMODE == 0) { k.r = xc * 4 + (i >> 3); k.c = i & 7; }
    else if (MAPMODE == 2) { k.r = (xc >> 1) * 8 + (i >> 2); k.c = (xc & 1) * 4 + (i & 3); }
    else { k.r = (xc >> 2) * 16 + (i >> 1); k.c = (xc & 3) * 2 + (i & 1); } }
  k.dead = false;
  f32x4 xres[3];
  v4u aphi[2];
#define PHASE_VARS()                                                             \
  k.fresh();                                                                     \
  const int w = k.w, r = k.r, c = k.c;                                           \
  const srd_t ws = mk_srd(k.ws, WS_END);                                         \
  const int ly = opqs(layer), ph0 = ly * PH_PER_LAYER, sb = 2 + ly * 64;         \
  const unsigned act = WS_ACT + (unsigned)ly * ACT_L;                            \
  const unsigned wt = WS_WT + (unsigned)c * WT_C;                                \
  (void)w; (void)r; (void)c; (void)ph0; (void)sb; (void)act; (void)wt;
  stamp(k, 0); stampc(k, 126);
  {
    const int w = k.w, r = k.r, c = k.c;
    const srd_t ws = mk_srd(wsp, WS_END);
    const unsigned wt = WS_WT + (unsigned)c * WT_C;
    const int lane = opq(lane0), g = lane >> 4, p = lane & 3, trr = tr_row(lane);
    if (r < 20) {
      const srd_t srd = mk_srd(wqkv + 160 * c + (size_t)r * 122880, 122880 - 160 * c);
#pragma unroll
      for (int u = 0; u < 2; ++u) {
        const unsigned id = (2 * w + u) * 64 + lane, row = (id * 2185u) >> 16, rem = id - row * 30u, sec = (rem * 6554u) >> 16;
        if (2 * w + u < 15) glds(L_S0 + (2 * w + u) * 1024u, row * 3840u + sec * 1280u + (rem - sec * 10u) * 16u, srd, 0);
      }
    }
    const int J = r >= 20 ? (r - 20) * 8 + w : 96 + r * 8 + w;
    const unsigned sj = L_SJ + w * 6144u;
    if (J < 108) {
      const unsigned char* W = J < 20 ? wo : J < 40 ? w1 : J < 60 ? w2 : w4;
      const int s = J < 20 ? J : J < 40 ? J - 20 : J < 60 ? J - 40 : J - 60;
      const srd_t srd = mk_srd(W + 160 * c + (size_t)s * 40960, 40960 - 160 * c);
#pragma unroll
      for (int j = 0; j < 5; ++j) {
        const unsigned t = 64 * j + lane, row = (t * 6554u) >> 16;
        glds(sj + j * 1024u, row * 1280u + (t - row * 10u) * 16u, srd, 0);
      }
    } else if (J < 148) {
      const int s = (J - 108) >> 1, half = (J - 108) & 1;
      const srd_t srd = mk_srd(w3 + 384 * c + 192 * half + (size_t)s * 98304, 98304 - 384 * c - 192 * half);
#pragma unroll
      for (int j = 0; j < 6; ++j) {
        const unsigned t = 64 * j + lane, row = (t * 5462u) >> 16;
        glds(sj + j * 1024u, row * 3072u + (t - row * 12u) * 16u, srd, 0);
      }
    }
    {
      const unsigned char* W; unsigned ld, col;
      if (k.b < 120) { W = wqkv; ld = 3840; col = k.b * 16; }
      else if (k.b < 160) { W = w1; ld = 1280; col = (k.b - 120) * 16; }
      else { W = w3; ld = 3072; col = (k.b - 160) * 16; }
      const unsigned vo = (unsigned)(lane >> 1) * ld + (lane & 1) * 16;
      const srd_t srd = mk_srd(W + col * 2, 640u * ld - col * 2);
#pragma unroll
      for (int u = 0; u < 3; ++u) {
        const int s = w + 8 * u;
        if (s < 20) glds(L_SCS + s * 1024u, vo, srd, s * 32u * ld);
      }
    }
    {
      u64 e = __hip_atomic_load((GAS u64*)(wsp + WS_EPOCH), __ATOMIC_RELAXED, __HIP_MEMORY_SCOPE_AGENT);
      const unsigned lo = __builtin_amdgcn_readfirstlane((unsigned)e), hi = __builtin_amdgcn_readfirstlane((unsigned)(e >> 32));
      k.tag = ((u64)hi << 32) | lo;
    }
    v4u xin0[10];
    {
      const GAS v4u* px = (const GAS v4u*)(x + (size_t)(r * 64 + (tid >> 3)) * 1280 + (tid & 7) * 16);
#pragma unroll
      for (int i = 0; i < 10; ++i) xin0[i] = px[8 * i];
    }
    {
      const int kh = w >> 2;
      const unsigned char* px = x + (size_t)(r * 64 + (w & 3) * 16 + (lane & 15)) * 1280 + (80 * c + 48 * kh + 4 * g) * 2;
#pragma unroll
      for (int j = 0; j < 3; ++j) {
        const v2u u = (j == 2 && kh) ? v2u{0u, 0u} : *(const GAS v2u*)(px + 32 * j);
        xres[j] = unpk(u);
      }
    }
    {
      const int row = lane & 15, h = 4 * c + (w >> 1);
      const GAS unsigned short* pw = (const GAS unsigned short*)wphi + h * 400;
#pragma unroll
      for (int fb = 0; fb < 2; ++fb) {
        const bool rowok = fb == 0 || (row & 3) == 0;
        const int f = 5 * (row >> 2) + (fb == 0 ? (row & 3) : 4);
        unsigned e[8];
#pragma unroll
        for (int j = 0; j < 8; ++j) {
          const bool ok = rowok && (j < 4 || g == 0);
          const int d = j < 4 ? 4 * g + j : 16 + (j - 4);
          const unsigned v = pw[(ok ? d : 0) * 20 + f];
          e[j] = ok ? v : 0u;
        }
        aphi[fb] = v4u{e[0] | (e[1] << 16), e[2] | (e[3] << 16), e[4] | (e[5] << 16), e[6] | (e[7] << 16)};
      }
    }
    BAR_VL0();
    stamp(k, 120);
    if (r < 20) {
      const int j = w >> 1, role = w & 1, vqp = role == 0 ? p : (p == 1 ? 0 : 4);
      const v4u f0 = trfrag<3840>(k.L + L_S0 + trr * 480 + role * 160 + 40 * j + 8 * p);
      const v4u f1 = trfrag<3840>(k.L + L_S0 + trr * 480 + 40 * j + (p == 0 ? role * 160 + 32 : 320 + 8 * vqp));
      st128(f0, lane * 16, ws, wt + WT_A + r * 16384u + w * 2048u);
      st128(f1, lane * 16, ws, wt + WT_A + r * 16384u + w * 2048u + 1024u);
      BAR_VL0();
      post_flag(k, fl_col(PH_T1, r, c), tag_of(k, PH_T1));
    }
    if (J < 108) {
      const int s = J < 20 ? J : J < 40 ? J - 20 : J < 60 ? J - 40 : J - 60;
      const int n = J < 60 ? 2 * (s % 10) + s / 10 : 2 * (s % 24) + s / 24;
      const unsigned dst = wt + (J < 20 ? WT_O : J < 40 ? WT_1 : J < 60 ? WT_2 : WT_4) + n * 5120u;
#pragma unroll
      for (int bl = 0; bl < 5; ++bl) st128(trfrag<1280>(k.L + sj + trr * 160 + p * 8 + 32 * bl), lane * 16, ws, dst + bl * 1024u);
    } else if (J < 148) {
      const int s = (J - 108) >> 1, half = (J - 108) & 1;
#pragma unroll
      for (int bl = 0; bl < 6; ++bl)
        st128(trfrag<1536>(k.L + sj + trr * 192 + p * 8 + 32 * bl), lane * 16, ws, wt + WT_3 + s * 12288u + (6 * half + bl) * 1024u);
    }
    {
      f32x4 cs = {0.f, 0.f, 0.f, 0.f};
      const v4u ones = {0x3F803F80u, 0x3F803F80u, 0x3F803F80u, 0x3F803F80u};
#pragma unroll
      for (int u = 0; u < 3; ++u) {
        const int s = w + 8 * u;
        if (s < 20) cs = mfma(trfrag<256>(k.L + L_SCS + s * 1024u + trr * 32 + p * 8), ones, cs);
      }
      if ((lane & 15) == 0) *(LAS f32x4*)(k.L + L_CSR + w * 64 + g * 16) = cs;
      BAR_L();
      if (w == 0) {
        if (lane < 16) {
          float s = 0.f;
#pragma unroll
          for (int u = 0; u < 8; ++u) s += *(LAS float*)(k.L + L_CSR + u * 64 + lane * 4);
          __hip_atomic_store((GAS float*)(wsp + WS_CS) + k.b * 16 + lane, s, __ATOMIC_RELAXED, __HIP_MEMORY_SCOPE_AGENT);
        }
        WAIT_VM(0);
        if (lane == 0) __hip_atomic_store((GAS u64*)(wsp + WS_FLAGS + PH_CS * 2048 + k.b * 8), tag_of(k, PH_CS), __ATOMIC_RELAXED, __HIP_MEMORY_SCOPE_AGENT);
      }
    }
    {
      const int tok = tid >> 3, part = tid & 7;
      float s = 0.f, q = 0.f;
#pragma unroll
      for (int i = 0; i < 10; ++i) {
        const int u = part + 8 * i;
        const int B = u >> 1, gq = (u & 1) * 2;
        LAS unsigned char* q0 = k.L + L_X + ((B >> 1) * 4 + (tok >> 4)) * 1024 + (tok & 15) * 16 + (B & 1) * 512 + (gq >> 1) * 8;
        *(LAS v2u*)(q0) = v2u{xin0[i].x, xin0[i].y};
        *(LAS v2u*)(q0 + 256) = v2u{xin0[i].z, xin0[i].w};
#pragma unroll
        for (int e = 0; e < 4; ++e) {
          const float a = bflo(xin0[i][e]), bb = bfhi(xin0[i][e]);
          s += a + bb; q = __builtin_fmaf(a, a, q); q = __builtin_fmaf(bb, bb, q);
        }
      }
      s += __shfl_xor(s, 1); q += __shfl_xor(q, 1); s += __shfl_xor(s, 2); q += __shfl_xor(q, 2); s += __shfl_xor(s, 4); q += __shfl_xor(q, 4);
      const float mean = s * (1.0f / 640.0f), var = __builtin_fmaxf(__builtin_fmaf(-mean, mean, q * (1.0f / 640.0f)), 0.0f);
      if (part == 0) *(LAS f32x2*)(k.L + L_STATS + tok * 8) = f32x2{mean, __builtin_amdgcn_rsqf(var + 1e-5f)};
    }
    BAR_VL0();
    post_flag(k, fl_col(PH_T2, r, c), tag_of(k, PH_T2));
  }
  stamp(k, 1);

  f32x4 x2r[3], x1r[3];

#pragma unroll 1
  for (int layer = 0; layer < 2; ++layer) {
    {
      PHASE_VARS()
      const int lane = opq(lane0), t = lane & 15, g = lane >> 4, j = w >> 1, role = w & 1;
      const unsigned v16 = pinv(lane * 16);
      const unsigned wa = pin(wt + WT_A + w * 2048u);
#define A_KS(i) (i)
#define A_WLOAD(i) { ld128<0>(wr[(i) % 5][0], v16, ws, wa + A_KS(i) * 16384u); ld128<1024>(wr[(i) % 5][1], v16, ws, wa + A_KS(i) * 16384u); }
#define A_WINDOW() { A_WLOAD(0) A_WLOAD(1) }
      const int hcol = (4 * c + j) * 20;
      const int vq = role == 0 ? g : (g == 1 ? 0 : 4);
      const unsigned col0 = pinv((role * 640 + hcol + 4 * g) * 4), col1 = pinv((g == 0 ? role * 640 + hcol + 16 : 1280 + hcol + 4 * vq) * 4);
      const unsigned xa = pin((unsigned)(w < 4 ? w : w + 6) * 4096u), xb2 = pin((unsigned)(w < 4 ? 8 + (w >> 1) : 16 + (w >> 1)) * 4096u + (w & 1) * 2048u);
      const unsigned xsrc = pin(WS_ACT + A_XO + (unsigned)r * 81920u);
      v4u wr[5][2];
#pragma unroll
      for (int i = 0; i < 5; ++i) asm volatile("" : "=v"(wr[i][0]), "=v"(wr[i][1]));
      if (FINE == 5 && ly == 1) stampw(k, 128);
      if (ly != 0 && w != 7) A_WINDOW()
      if (FINE == 5 && ly == 1) { WAIT_VM(0); stampw(k, 136); }
      if (ly == 0) poll_flags(k, fl_col(PH_T1, (lane & 31) < 20 ? (lane & 31) : 0, c), tag_of(k, PH_T1));
      else poll_flags(k, fl_group(PH_XO, r, lane & 7), tag_of(k, PH_XO));
      BAR();
      if (ly == 0) {
        A_WINDOW()
        const int f = lane < 15 ? (lane / 5) * 40 + 5 * c + lane % 5 : lane < 20 ? 120 + 5 * c + (lane - 15) : 160 + 12 * c + ((lane - 20) % 12);
        poll_flags(k, PH_CS * 2048 + f * 8, tag_of(k, PH_CS));
        poll_flags(k, fl_col(PH_T2, lane & 31, c), tag_of(k, PH_T2));
        BAR_VL0();
      } else {
        FST(42)
        gln<4>(L_X + xa, v16, ws, xsrc + xa); gln<4>(L_X + xa + 16384u, v16, ws, xsrc + xa + 16384u); gln<2>(L_X + xb2, v16, ws, xsrc + xb2);
        if (w == 7) A_WINDOW()
        if (FINE == 5) { WAIT_VM(0); stampw(k, 144); }
        if (w < 4) WAIT_VM(6); else if (w < 7) WAIT_VM(10);
        BAR();
      }
      v4u cs0, cs1;
      StatLd st;
      A_WLOAD(2) A_WLOAD(3) A_WLOAD(4)
      stamp(k, sb + 0);
      f32x4 acc[2][4];
#pragma unroll
      for (int bl = 0; bl < 2; ++bl)
#pragma unroll
        for (int tb = 0; tb < 4; ++tb) acc[bl][tb] = f32x4{0.f, 0.f, 0.f, 0.f};
      v4u xf[2][4];
      LAS unsigned char* xb = k.L + L_X + v16;
#define A_READ(i) { _Pragma("unroll") for (int tb = 0; tb < 4; ++tb) xf[(i) & 1][tb] = *(LAS v4u*)(xb + ((i) * 4 + tb) * 1024); }
#define A_IT(i, N)                                                                                    \
  {                                                                                                   \
    wait_tie2<N>(wr[(i) % 5][0], wr[(i) % 5][1]);                                                     \
    if ((i) == 3) BAR();                    \
    if ((i) + 1 < 20) A_READ((i) + 1)                                                                 \
    _Pragma("unroll") for (int tb = 0; tb < 4; ++tb) {                                                \
      acc[0][tb] = mfma(wr[(i) % 5][0], xf[(i) & 1][tb], acc[0][tb]);                                 \
      acc[1][tb] = mfma(wr[(i) % 5][1], xf[(i) & 1][tb], acc[1][tb]);                                 \
    }                                                                                                 \
    if ((i) + 1 < 20) MIX(2, 4)                                                                       \
    SB();                                                                                             \
    if ((i) + 5 < 20) A_WLOAD((i) + 5)                                                                \
    if ((i) == 1) { stat_issue(st, ws, 2, r * 4 + (w & 3), lane);        \
                    ld128<0>(cs0, col0, ws, WS_CS); ld128<0>(cs1, col1, ws, WS_CS); }                 \
  }
      FST(20)
      A_READ(0)
#define A_W7() if (w == 7) WAIT_VM(8);
      A_W7() A_IT(0, 63) A_W7() A_IT(1, 63) A_IT(2, 14) A_IT(3, 14) FST(21) A_IT(4, 14) A_IT(5, 14) A_IT(6, 14) A_IT(7, 8) FST(22)
      A_IT(8, 8) A_IT(9, 8) A_IT(10, 8) A_IT(11, 8) FST(23) A_IT(12, 8) A_IT(13, 8) A_IT(14, 8) A_IT(15, 8) FST(24)
      A_IT(16, 6) A_IT(17, 4) A_IT(18, 2) A_IT(19, 0)
#undef A_IT
#undef A_W7
#undef A_READ
#undef A_WINDOW
#undef A_WLOAD
#undef A_KS
      if (ly) {
        wait_tie_stat<0>(st);
        float mean, var; stat_finish(st, mean, var);
        if (lane < 16) *(LAS f32x2*)(k.L + L_STATS + ((w & 3) * 16 + lane) * 8) = f32x2{mean, __builtin_amdgcn_rsqf(var + 1e-5f)};
      }
      BAR_L();
      stamp(k, sb + 1);
      tie2(cs0, cs1);
      const f32x4 c0 = __builtin_bit_cast(f32x4, cs0), c1 = __builtin_bit_cast(f32x4, cs1);
      unsigned phq[4][5];
      LAS unsigned char* img = k.L + L_IMG + j * IMG_H;
      {
        v4u bfr[4];
        f32x2 ms[4];
#pragma unroll
        for (int tb = 0; tb < 4; ++tb) ms[tb] = *(LAS f32x2*)(k.L + L_STATS + (tb * 16 + t) * 8);
#pragma unroll
        for (int tb = 0; tb < 4; ++tb) {
          const float nm = -ms[tb].x, rs = ms[tb].y;
          const f32x4 a0 = acc[0][tb], a1 = acc[1][tb];
          bfr[tb] = v4u{pk2v((f32x2{c0.x, c0.y} * nm + f32x2{a0.x, a0.y}) * rs), pk2v((f32x2{c0.z, c0.w} * nm + f32x2{a0.z, a0.w}) * rs),
                        pk2v((f32x2{c1.x, c1.y} * nm + f32x2{a1.x, a1.y}) * rs), pk2v((f32x2{c1.z, c1.w} * nm + f32x2{a1.z, a1.w}) * rs)};
        }
#define WO_GROUP(u) { const int g_ = (u) * 8 + w; if (g_ < 25) gln<4>((unsigned)(P_C * 5 + 4 * g_) * 1024u, v16, ws, wt + WT_O + (unsigned)g_ * 4096u); }
        WO_GROUP(0)
        f32x4 pa[4], pb[4];
        const f32x4 z = {0.f, 0.f, 0.f, 0.f};
#pragma unroll
        for (int tb = 0; tb < 4; ++tb) { pa[tb] = mfma(aphi[0], bfr[tb], z); pb[tb] = mfma(aphi[1], bfr[tb], z); }
        {
          const int vslot = role == 0 ? (g == 0 ? 6 : g) : (g == 0 ? 7 : g == 1 ? 0 : g == 2 ? 4 : 5);
          const bool ones = role == 1 && g == 3;
          LAS unsigned char* vrow = img + t * 160 + 96 + 8 * vslot;
#pragma unroll
          for (int tb = 0; tb < 4; ++tb) *(LAS v2u*)(vrow + tb * 2560) = ones ? v2u{0x00003F80u, 0u} : v2u{bfr[tb].z, bfr[tb].w};
        }
        SB();
        constexpr float L2E = 1.4426950408889634f;
        f32x2 p01[4], p23[4]; float p4[4], mx[4], mn[4];
#pragma unroll
        for (int tb = 0; tb < 4; ++tb) {
          p01[tb] = f32x2{pa[tb].x, pa[tb].y} * L2E; p23[tb] = f32x2{pa[tb].z, pa[tb].w} * L2E; p4[tb] = pb[tb].x * L2E;
          mx[tb] = fmax3_raw(fmax3_raw(p01[tb].x, p01[tb].y, p23[tb].x), p23[tb].y, p4[tb]);
          mn[tb] = -fmin3_raw(fmin3_raw(p01[tb].x, p01[tb].y, p23[tb].x), p23[tb].y, p4[tb]);
        }
        xr4<true>(mx, mn);
        SB();
        WO_GROUP(1)
        float e[4][5], f[4][5], sp[4], sm[4];
#pragma unroll
        for (int tb = 0; tb < 4; ++tb) {
          const f32x2 a = p01[tb] - mx[tb], b = p23[tb] - mx[tb], cc = p01[tb] + mn[tb], d = p23[tb] + mn[tb];
          e[tb][0] = __builtin_amdgcn_exp2f(a.x); e[tb][1] = __builtin_amdgcn_exp2f(a.y); e[tb][2] = __builtin_amdgcn_exp2f(b.x);
          e[tb][3] = __builtin_amdgcn_exp2f(b.y); e[tb][4] = __builtin_amdgcn_exp2f(p4[tb] - mx[tb]);
          f[tb][0] = __builtin_amdgcn_exp2f(-cc.x); f[tb][1] = __builtin_amdgcn_exp2f(-cc.y); f[tb][2] = __builtin_amdgcn_exp2f(-d.x);
          f[tb][3] = __builtin_amdgcn_exp2f(-d.y); f[tb][4] = __builtin_amdgcn_exp2f(-(p4[tb] + mn[tb]));
        }
#pragma unroll
        for (int tb = 0; tb < 4; ++tb) {
          const f32x2 s2 = (f32x2{e[tb][0], e[tb][1]} + f32x2{e[tb][2], e[tb][3]}), s3 = (f32x2{f[tb][0], f[tb][1]} + f32x2{f[tb][2], f[tb][3]});
          sp[tb] = (s2.x + s2.y) + e[tb][4]; sm[tb] = (s3.x + s3.y) + f[tb][4];
        }
        xr4<false>(sp, sm);
        SB();
        WO_GROUP(2)
#pragma unroll
        for (int tb = 0; tb < 4; ++tb) {
          const float rp = __builtin_amdgcn_rcpf(sp[tb]), rm = __builtin_amdgcn_rcpf(sm[tb]);
          phq[tb][0] = pk2v(f32x2{e[tb][0], e[tb][1]} * rp); phq[tb][1] = pk2v(f32x2{e[tb][2], e[tb][3]} * rp);
          phq[tb][2] = pk2(e[tb][4] * rp, f[tb][0] * rm);
          phq[tb][3] = pk2v(f32x2{f[tb][1], f[tb][2]} * rm); phq[tb][4] = pk2v(f32x2{f[tb][3], f[tb][4]} * rm);
        }
        WO_GROUP(3)
#undef WO_GROUP
        if (role == 1) {
          LAS unsigned char* row = img + t * 160 + 24 * g;
#pragma unroll
          for (int tb = 0; tb < 4; ++tb) {
            *(LAS v2u*)(row + tb * 2560) = v2u{phq[tb][0], phq[tb][1]};
            *(LAS v2u*)(row + tb * 2560 + 8) = v2u{phq[tb][2], phq[tb][3]};
            *(LAS v2u*)(row + tb * 2560 + 16) = v2u{phq[tb][4], 0u};
          }
        }
      }
      FST(25)
      BAR_L();
      {
        const unsigned lb = tr_row(lane) * 160 + (lane & 3) * 8;
        f32x4 kv[3];
#pragma unroll
        for (int cb = 0; cb < 3; ++cb) kv[cb] = f32x4{0.f, 0.f, 0.f, 0.f};
#pragma unroll
        for (int ks = 0; ks < 2; ++ks) {
          const v4u bv = trfrag<1280>(img + lb + ks * 5120 + 96 + 32 * role);
#pragma unroll
          for (int cb = 0; cb < 3; ++cb) kv[cb] = mfma(trfrag<1280>(img + lb + ks * 5120 + 32 * cb), bv, kv[cb]);
        }
        const unsigned so = WS_PART + ly * PART_L + (unsigned)(c * 32 + r) * 16384u + j * 4096u;
        const unsigned vo2 = role == 0 ? v16 : (t < 5 ? (unsigned)(3072 + (g * 5 + t) * 16) : OOB);
        const unsigned st = role == 0 ? 1024u : 320u;
        const u64 tg = tag_of(k, ph0 + PH_KV1);
#pragma unroll
        for (int cb = 0; cb < 3; ++cb) st128(granule(kv[cb], tg), vo2, ws, so + cb * st);
      }
      FST(26)
      stamp(k, sb + 2);
      if (w == 7) {
        const unsigned so = WS_PART + ly * PART_L + (unsigned)(c * 32) * 16384u + r * 512u;
        const unsigned vo2 = (unsigned)((lane >> 5) * 16384 + (lane & 31) * 16);
        const bool hole = (r & 7) == 7 && (lane & 31) >= 28;
        const u64 tg = tag_of(k, ph0 + PH_KV1);
        v4u pr[16];
        if (FINE == 4 && ly == 1) stampw(k, 128);
        unsigned spins = 0;
        for (;;) {
          bool ok = true;
#pragma unroll
          for (int i = 0; i < 16; ++i) pr[i] = __builtin_amdgcn_raw_buffer_load_b128(ws, vo2, so + i * 32768u, 16);
#pragma unroll
          for (int i = 0; i < 16; ++i) ok = ok && granule_ok(pr[i], tg);
          if (__all(ok || hole) || !spin_more(k, spins)) break;
        }
        if (FINE == 4 && ly == 1) { stampw(k, 136); if (lane == 0) *(LAS u64*)(k.L + L_MISC + 2048 + 152 * 8) = spins; }
        f32x4 sum = {0.f, 0.f, 0.f, 0.f};
#pragma unroll
        for (int i = 0; i < 16; ++i) sum += f32x4{bflo(pr[i].x), bfhi(pr[i].x), bflo(pr[i].z), bfhi(pr[i].z)};
#pragma unroll
        for (int e = 0; e < 4; ++e) {
          auto sw = __builtin_amdgcn_permlane32_swap(asu(sum[e]), asu(sum[e]), false, false);
          sum[e] = asf(sw[0]) + asf(sw[1]);
        }
        st128(granule(sum, tag_of(k, ph0 + PH_KV2)), lane < 32 ? v16 : OOB, ws, WS_RED + (unsigned)(ly * 8 + c) * 16384u + r * 512u);
        stamp(k, sb + 3);
      } else if (role == 0) {
        const unsigned so = WS_RED + (unsigned)(ly * 8 + c) * 16384u + j * 4096u;
        const int dd = t < 4 ? t : ((t & 3) == 0 ? 4 : -1);
        const int Q0 = 20 * c + 5 * j + g, Q1 = 20 * c + 5 * j + 4;
        const unsigned o0 = pinv(frag_off(Q0 >> 2, Q0 & 3, t)), o1 = pinv(g == 0 ? frag_off(Q1 >> 2, Q1 & 3, t) : OOB);
        const u64 tg = tag_of(k, ph0 + PH_KV2);
        v4u l0[3], l1[3];
        if (FINE == 4 && ly == 1) stampw(k, 128);
        unsigned spins = 0;
        for (;;) {
          bool ok = true;
#pragma unroll
          for (int u = 0; u < 3; ++u) {
            const int qc = 3 * g + u;
            l0[u] = __builtin_amdgcn_raw_buffer_load_b128(ws, (unsigned)((qc >> 2) * 1024 + ((qc & 3) * 16 + t) * 16), so, 16);
            l1[u] = __builtin_amdgcn_raw_buffer_load_b128(ws, dd >= 0 ? (unsigned)(3072 + (qc * 5 + dd) * 16) : OOB, so, 16);
          }
#pragma unroll
          for (int u = 0; u < 3; ++u) ok = ok && granule_ok(l0[u], tg) && (dd < 0 || granule_ok(l1[u], tg));
          if (__all(ok) || !spin_more(k, spins)) break;
        }
        if (FINE == 4 && ly == 1) { stampw(k, 136); if (lane == 0 && w == 0) *(LAS u64*)(k.L + L_MISC + 2048 + 153 * 8) = spins; }
        stamp(k, sb + 3);
        const v4u a00 = {l0[0].x, l0[0].z, l0[1].x, l0[1].z}, a01 = {l0[2].x, l0[2].z, 0u, 0u};
        const v4u a10 = {l1[0].x, l1[0].z, l1[1].x, l1[1].z}, a11 = {l1[2].x, l1[2].z, 0u, 0u};
        f32x4 n0[4], n1[4];
        const f32x4 z = {0.f, 0.f, 0.f, 0.f};
#pragma unroll
        for (int tb = 0; tb < 4; ++tb) {
          const v4u b0 = {phq[tb][0], phq[tb][1], phq[tb][2], phq[tb][3]}, b1 = {phq[tb][4], 0u, 0u, 0u};
          n0[tb] = mfma(a01, b1, mfma(a00, b0, z)); n1[tb] = mfma(a11, b1, mfma(a10, b0, z));
        }
#pragma unroll
        for (int tb = 0; tb < 4; ++tb) {
          auto sw = __builtin_amdgcn_permlane16_swap(asu(n1[tb].x), asu(n1[tb].x), false, false);
          const float rd = __builtin_amdgcn_rcpf((g == 0 ? asf(sw[1]) : n1[tb].x) + 1e-6f);
          const unsigned so2 = act + A_O + (unsigned)r * 81920u + tb * 1024u;
          st64(pk4(n0[tb] * rd), o0, ws, so2);
          st64(pk4(n1[tb] * rd), o1, ws, so2);
        }
        WAIT_VM(0);
      }
      BAR();
      post_flag(k, fl_group(ph0 + PH_O, r, c), tag_of(k, ph0 + PH_O));
      stamp(k, sb + 4);
    }
    if (STOP_PHASE == layer * 7 + 0) return;
    {
      PHASE_VARS()
      const int lane = opq(lane0);
      const unsigned v16 = pinv(lane * 16), xo = kx_off(k, act + A_O);
      const RingAddr ra = ring_addr(k, lane);
      const unsigned pa1 = (unsigned)(2 * w < 13 ? 2 * w : 13) * 1024u, pl1 = pin(140u * 1024u + pa1), ps1 = pin(wt + WT_1 + pa1);
      const unsigned pl2 = pin(5u * w * 1024u), ps2 = pin(wt + WT_1 + (15u + 5u * w) * 1024u);
      const unsigned pa3 = (unsigned)(6 * w < 39 ? 6 * w : 39) * 1024u, pl3 = pin(40u * 1024u + pa3), ps3 = pin(wt + WT_1 + 55u * 1024u + pa3);
      poll_flags(k, fl_group(ph0 + PH_O, r, lane & 7), tag_of(k, ph0 + PH_O));
      BAR_VL0();
      stamp(k, sb + 5);
      v4u xf[10];
      kx_issue(xf, ws, xo, v16);
      f32x4 acc[5];
#pragma unroll
      for (int bl = 0; bl < 5; ++bl) acc[bl] = f32x4{0.f, 0.f, 0.f, 0.f};
#define C_PF(p) { if ((p) < 2) gl1<(p) & 1>(pl1, v16, ws, ps1); else gl1<((p) >= 2 ? (p) - 2 : 0)>(pl2, v16, ws, ps2); }
      KMAIN(P_C, 0, 7, C_PF)
#undef C_PF
      BAR_L();
      stamp(k, sb + 6);
      gln<2>(pl3, v16, ws, ps3);
      f32x4 fin[3];
      kexchange(k, acc, fin, 17 * SLOT, lane);
      gln<2>(pl3 + 2048, v16, ws, ps3 + 2048);
      v2u pk[3];
#pragma unroll
      for (int j = 0; j < 3; ++j) { pk[j] = pk4(xres[j] + rnd4(fin[j])); x2r[j] = unpk(pk[j]); }
      gln<2>(pl3 + 4096, v16, ws, ps3 + 4096);
      kstore(k, ws, act + A_X2, pk, lane);
      kstats(k, ws, ly * 3 + 0, x2r, lane);
      FST(28)
      BAR_VL0();
      post_flag(k, fl_group(ph0 + PH_X2, r, c), tag_of(k, ph0 + PH_X2));
      stamp(k, sb + 7);
    }
    if (STOP_PHASE == layer * 7 + 1) return;
    {
      PHASE_VARS()
      const int lane = opq(lane0), t = lane & 15, g = lane >> 4, kh = w >> 2;
      const unsigned v16 = pinv(lane * 16), xo = kx_off(k, act + A_X2);
      const unsigned co = pinv((unsigned)(1920 + 80 * c + 48 * kh + 4 * g) * 4u);
      const RingAddr ra = ring_addr(k, lane);
      const int shift = (c >> 1) == 0 ? 1 : (c >> 1) == 1 ? -1 : (c >> 1) == 2 ? 2 : -2;
      const int tok = r * 64 + (w & 3) * 16 + t, dst = tok + shift;
      const unsigned dof = pinv((dst >= 0 && dst < S_) ? (unsigned)(dst >> 6) * 81920u + (unsigned)((dst >> 4) & 3) * 1024u + frag_off(0, g, dst & 15) : OOB);
      const unsigned pa1 = (unsigned)(7 * w < 48 ? 7 * w : 48) * 1024u, pl1 = pin(85u * 1024u + pa1), ps1 = pin(wt + WT_2 + pa1);
      const unsigned pa2 = (unsigned)(2 * w < 13 ? 2 * w : 13) * 1024u, pl2 = pin(140u * 1024u + pa2), ps2 = pin(wt + WT_2 + 55u * 1024u + pa2);
      const unsigned pa3 = (unsigned)(4 * w < 26 ? 4 * w : 26) * 1024u, pl3 = pin(pa3), ps3 = pin(wt + WT_2 + 70u * 1024u + pa3);
      poll_flags(k, fl_group(ph0 + PH_X2, r, lane & 7), tag_of(k, ph0 + PH_X2));
      BAR();
      stamp(k, sb + 8);
      v4u xf[10];
      kx_issue(xf, ws, xo, v16);
      StatLd st; stat_issue(st, ws, ly * 3 + 0, r * 4 + (w & 3), lane);
      v4u cs[3];
      ld128<0>(cs[0], co, ws, WS_CS); ld128<64>(cs[1], co, ws, WS_CS); ld128<128>(cs[2], co, ws, WS_CS);
      f32x4 acc[5];
#pragma unroll
      for (int bl = 0; bl < 5; ++bl) acc[bl] = f32x4{0.f, 0.f, 0.f, 0.f};
#define D_PF(p) gl1<p>(pl1, v16, ws, ps1);
      KMAIN(P_D, 7, 7, D_PF)
#undef D_PF
      asm volatile("s_waitcnt vmcnt(7)" : "+v"(st.s0), "+v"(st.s1), "+v"(st.s2), "+v"(st.s3), "+v"(cs[0]), "+v"(cs[1]), "+v"(cs[2]));
      float mean, var; stat_finish(st, mean, var);
      const float rstd = __builtin_amdgcn_rsqf(var + 1e-5f);
      const float sc2 = rstd * __builtin_amdgcn_rsqf(var * rstd * rstd + 1e-5f);
      BAR_L();
      stamp(k, sb + 9);
      gln<2>(pl2, v16, ws, ps2);
      f32x4 fin[3];
      kexchange(k, acc, fin, 6 * SLOT, lane);
      gln<2>(pl3, v16, ws, ps3);
#pragma unroll
      for (int j = 0; j < 3; ++j) {
        const int B = 5 * c + 3 * kh + j;
        if (!(j == 2 && kh)) st64(ln_gelu4(fin[j], __builtin_bit_cast(f32x4, cs[j]), -mean, sc2), dof, ws, act + A_HS + blk_off(B));
      }
      gln<2>(pl3 + 2048, v16, ws, ps3 + 2048);
      if (r == 0 || r == 31) {
        const bool z = shift > 0 ? tok < shift : tok >= S_ + shift;
        const unsigned zof = z ? (unsigned)(tok >> 6) * 81920u + (unsigned)((tok >> 4) & 3) * 1024u + frag_off(0, g, tok & 15) : OOB;
#pragma unroll
        for (int j = 0; j < 3; ++j) {
          const int B = 5 * c + 3 * kh + j;
          if (!(j == 2 && kh)) st64(v2u{0u, 0u}, zof, ws, act + A_HS + blk_off(B));
        }
      }
#pragma unroll
      for (int j = 0; j < 3; ++j) x1r[j] = rnd4((x2r[j] - mean) * rstd);
      BAR_VL0();
      post_flag(k, fl_group(ph0 + PH_H, r, c), tag_of(k, ph0 + PH_H));
      stamp(k, sb + 10);
    }
    if (STOP_PHASE == layer * 7 + 2) return;
#define F_WLOAD(i) { ld128<0>(wf3[(i) % 4][0], v16, ws, wa3 + (i) * 12288u); ld128<1024>(wf3[(i) % 4][1], v16, ws, wa3 + (i) * 12288u); \
                     ld128<2048>(wf3[(i) % 4][2], v16, ws, wa3 + (i) * 12288u); }
    {
      PHASE_VARS()
      const int lane = opq(lane0);
      const unsigned v16 = pinv(lane * 16), xo = kx_off(k, act + A_HS);
      const RingAddr ra = ring_addr(k, lane);
      const unsigned pa1 = (unsigned)(6 * w < 39 ? 6 * w : 39) * 1024u, pl1 = pin(P_G * 5u * 1024u + pa1), ps1 = pin(wt + WT_4 + pa1);
      { const int rr = r + ((lane >> 3) % 3) - 1;
        poll_flags(k, fl_group(ph0 + PH_H, rr < 0 ? 0 : (rr > 31 ? 31 : rr), lane & 7), tag_of(k, ph0 + PH_H)); }
      BAR();
      stamp(k, sb + 11);
      v4u xf[10];
      kx_issue(xf, ws, xo, v16);
      f32x4 acc[5];
#pragma unroll
      for (int bl = 0; bl < 5; ++bl) acc[bl] = f32x4{0.f, 0.f, 0.f, 0.f};
#define E_PF(p) gl1<p>(pl1, v16, ws, ps1);
      KMAIN(P_E, 0, 6, E_PF)
#undef E_PF
      BAR_L();
      stamp(k, sb + 12);
      f32x4 fin[3];
      kexchange(k, acc, fin, 17 * SLOT, lane);
      v2u pk[3];
#pragma unroll
      for (int j = 0; j < 3; ++j) { pk[j] = pk4(x1r[j] + rnd4(fin[j])); x1r[j] = unpk(pk[j]); }
      kstore(k, ws, act + A_X1, pk, lane);
      kstats(k, ws, ly * 3 + 1, x1r, lane);
      BAR_VL0();
      post_flag(k, fl_group(ph0 + PH_X1, r, c), tag_of(k, ph0 + PH_X1));
      stamp(k, sb + 13);
    }
    if (STOP_PHASE == layer * 7 + 3) return;
    {
      PHASE_VARS()
      const int lane = opq(lane0), t = lane & 15, g = lane >> 4, cp = w & 3, kh = w >> 2;
      const unsigned v16 = pinv(lane * 16);
      const unsigned wa3 = pin(wt + WT_3 + (unsigned)(kh * 10) * 12288u + cp * 3072u);
      const unsigned co = pinv((unsigned)(2560 + 192 * c + 48 * cp + 4 * g) * 4u);
      const int ka = (w & 1) + ((w >> 1) & 1) * 10 + (w >> 2) * 5;
      const unsigned xa = pin((unsigned)ka * 4096u), xb2 = pin((unsigned)(4 + ((w >> 1) & 1) * 10 + (w >> 2) * 5) * 4096u + (w & 1) * 2048u);
      const unsigned xsrc = pin(act + A_X1 + (unsigned)r * 81920u);
      v4u wf3[4][3];
#pragma unroll
      for (int i = 0; i < 4; ++i) asm volatile("" : "=v"(wf3[i][0]), "=v"(wf3[i][1]), "=v"(wf3[i][2]));
#define F_WINDOW() { F_WLOAD(0) }
      if (w != 7) F_WINDOW()
      poll_flags(k, fl_group(ph0 + PH_X1, r, lane & 7), tag_of(k, ph0 + PH_X1));
      BAR();
      FST(41)
      gln<4>(L_XF + xa, v16, ws, xsrc + xa); gln<4>(L_XF + xa + 8192u, v16, ws, xsrc + xa + 8192u); gln<2>(L_XF + xb2, v16, ws, xsrc + xb2);
      if (w == 7) F_WINDOW()
#undef F_WINDOW
      f32x4 acc[3][4];
#pragma unroll
      for (int bl = 0; bl < 3; ++bl)
#pragma unroll
        for (int tb = 0; tb < 4; ++tb) acc[bl][tb] = f32x4{0.f, 0.f, 0.f, 0.f};
      if (w < 4) WAIT_VM(6); else if (w < 7) WAIT_VM(10);
      BAR();
      StatLd st;
      v4u cs[3];
      F_WLOAD(1) F_WLOAD(2) F_WLOAD(3)
      stamp(k, sb + 14);
      FST(29)
      v4u xf[2][4];
      LAS unsigned char* xb = k.L + L_XF + v16 + kh * 40960;
#define F_READ(i) { _Pragma("unroll") for (int tb = 0; tb < 4; ++tb) xf[(i) & 1][tb] = *(LAS v4u*)(xb + ((i) * 4 + tb) * 1024); }
#define F_IT(i, N)                                                                                    \
  {                                                                                                   \
    wait_tie3<N>(wf3[(i) % 4][0], wf3[(i) % 4][1], wf3[(i) % 4][2]);                                  \
    if ((i) == 1) BAR();                    \
    if ((i) + 1 < 10) F_READ((i) + 1)                                                                 \
    _Pragma("unroll") for (int tb = 0; tb < 4; ++tb)                                                  \
      _Pragma("unroll") for (int bl = 0; bl < 3; ++bl) acc[bl][tb] = mfma(wf3[(i) % 4][bl], xf[(i) & 1][tb], acc[bl][tb]); \
    if ((i) + 1 < 10) MIX(3, 4)                                                                       \
    SB();                                                                                             \
    if ((i) + 4 < 10) F_WLOAD((i) + 4)                                                                \
    if ((i) == 1) { stat_issue(st, ws, ly * 3 + 1, r * 4 + (w & 3), lane);                            \
                    ld128<0>(cs[0], co, ws, WS_CS); ld128<64>(cs[1], co, ws, WS_CS); ld128<128>(cs[2], co, ws, WS_CS); } \
  }
      F_READ(0)
#define F_W7() if (w == 7) WAIT_VM(9);
      F_W7() F_IT(0, 63) F_IT(1, 9) FST(30) F_IT(2, 16) FST(31) F_IT(3, 16) F_IT(4, 16) FST(32) F_IT(5, 16) F_IT(6, 9) F_IT(7, 6) F_IT(8, 3) F_IT(9, 0)
#undef F_IT
#undef F_W7
#undef F_READ
      {
        wait_tie_stat<0>(st);
        float mean, var; stat_finish(st, mean, var);
        if (lane < 16) *(LAS f32x2*)(k.L + L_STATS + ((w & 3) * 16 + lane) * 8) = f32x2{mean, __builtin_amdgcn_rsqf(var + 1e-5f)};
      }
      BAR_L();
      stamp(k, sb + 15);
      f32x4 fin[3][2];
      {
        LAS f32x4* sc = (LAS f32x4*)(k.L + L_XF + v16);
        wstream_exact<4>(k, ws, wt + WT_4 + 125 * 1024, 0, 30, v16);
        if (kh == 0) {
#pragma unroll
          for (int bl = 0; bl < 3; ++bl) { sc[((cp * 2 + 1) * 6 + bl * 2) * 64] = acc[bl][2]; sc[((cp * 2 + 1) * 6 + bl * 2 + 1) * 64] = acc[bl][3];
                                           fin[bl][0] = acc[bl][0]; fin[bl][1] = acc[bl][1]; }
        } else {
#pragma unroll
          for (int bl = 0; bl < 3; ++bl) { sc[((cp * 2) * 6 + bl * 2) * 64] = acc[bl][0]; sc[((cp * 2) * 6 + bl * 2 + 1) * 64] = acc[bl][1];
                                           fin[bl][0] = acc[bl][2]; fin[bl][1] = acc[bl][3]; }
        }
        BAR_L();
#pragma unroll
        for (int bl = 0; bl < 3; ++bl) { fin[bl][0] += sc[((cp * 2 + kh) * 6 + bl * 2) * 64]; fin[bl][1] += sc[((cp * 2 + kh) * 6 + bl * 2 + 1) * 64]; }
        BAR_L();
      }
      FST(33)
      const unsigned w4l = pin((75u + 10u * w) * 1024u), w4g = pin(wt + WT_4 + (45u + 10u * w) * 1024u);
      tie3(cs[0], cs[1], cs[2]);
      const unsigned lo = frag_off(0, g, t);
#pragma unroll
      for (int u = 0; u < 2; ++u) {
        const int tb = 2 * kh + u;
        const f32x2 ms = *(LAS f32x2*)(k.L + L_STATS + (tb * 16 + t) * 8);
#pragma unroll
        for (int bl = 0; bl < 3; ++bl) {
          const int B = 12 * c + 3 * cp + bl;
          if (u == 0 && bl == 0) gln<4>(w4l, v16, ws, w4g);
          if (u == 0 && bl == 2) gln<4>(w4l + 4096, v16, ws, w4g + 4096);
          if (u == 1 && bl == 1) gln<2>(w4l + 8192, v16, ws, w4g + 8192);
          st64(ln_gelu4(fin[bl][u], __builtin_bit_cast(f32x4, cs[bl]), -ms.x, ms.y), lo, ws,
               act + A_H2 + (unsigned)r * 196608u + tb * 1024u + blk_off(B));
        }
      }
      FST(34)
      BAR_VL0();
      post_flag(k, fl_group(ph0 + PH_H2, r, c), tag_of(k, ph0 + PH_H2));
      stamp(k, sb + 16);
    }
#undef F_WLOAD
    if (STOP_PHASE == layer * 7 + 4) return;
    {
      PHASE_VARS()
      const int lane = opq(lane0), t = lane & 15, g = lane >> 4, kh = w >> 2;
      const unsigned v16 = pinv(lane * 16);
      const unsigned xo = pin(act + A_H2 + (unsigned)r * 196608u + (unsigned)kh * 98304u + (unsigned)(w & 3) * 1024u);
      const RingAddr ra = ring_addr(k, lane);
      const unsigned w4s = pin(wt + WT_4 + (unsigned)w * SLOT);
      poll_flags(k, fl_group(ph0 + PH_H2, r, lane & 7), tag_of(k, ph0 + PH_H2));
      BAR();
      stamp(k, sb + 17);
      v4u xf[24];
#define GX(i) ld128<0>(xf[i], v16, ws, xo + (i) * 4096u);
      GX(0) GX(1) GX(2) GX(3) GX(4) GX(5) GX(6) GX(7)
      f32x4 acc[5];
#pragma unroll
      for (int bl = 0; bl < 5; ++bl) acc[bl] = f32x4{0.f, 0.f, 0.f, 0.f};
      v4u wf[2][5];
#define G_MMA(i) { _Pragma("unroll") for (int bl = 0; bl < 5; ++bl) acc[bl] = mfma(wf[(i) & 1][bl], xf[i], acc[bl]); }
#define G_STEP(i)                                                                                     \
  {                                                                                                   \
    if ((i) >= 1) wait_tie<g_xwait((i) >= 1 ? (i) : 1)>(xf[(i) >= 1 ? (i) - 1 : 0]);                  \
    ring_read<P_G + 2 * (i)>(ra, wf[(i) & 1]);                                                        \
    if ((i) >= 1) { G_MMA((i) >= 1 ? (i) - 1 : 0) MIX(1, 5) }                                         \
    SB();                                                                                             \
    if ((i) + 8 < 24) GX((i) + 8 < 24 ? (i) + 8 : 0)                                                  \
  }
      G_STEP(0) G_STEP(1) FST(35) G_STEP(2) G_STEP(3)
      FST(36)
      BAR_L();
      {
        const unsigned l = (unsigned)(6 + w) * SLOT, g = w4s + 31 * SLOT;
        G_STEP(4) gl1<0>(l, v16, ws, g); gl1<1>(l, v16, ws, g); G_STEP(5) gl1<2>(l, v16, ws, g); G_STEP(6) gl1<3>(l, v16, ws, g); G_STEP(7) gl1<4>(l, v16, ws, g);
      }
      BAR_L();
      {
        const unsigned l = (unsigned)(14 + w) * SLOT, g = w4s + 39 * SLOT;
        G_STEP(8) gl1<0>(l, v16, ws, g); gl1<1>(l, v16, ws, g); G_STEP(9) gl1<2>(l, v16, ws, g); G_STEP(10) gl1<3>(l, v16, ws, g); G_STEP(11) gl1<4>(l, v16, ws, g);
      }
      FST(37)
      bar_vl<9>();
      if (w == 0) gln<5>(22 * SLOT, v16, ws, w4s + 47 * SLOT);
      G_STEP(12) G_STEP(13) G_STEP(14) G_STEP(15)
      bar_vl<4>();
      G_STEP(16) G_STEP(17) G_STEP(18) G_STEP(19)
      FST(38)
      bar_vl<0>();
      G_STEP(20) G_STEP(21) G_STEP(22) G_STEP(23)
      tie1(xf[23]);
      G_MMA(23)
#undef G_STEP
#undef G_MMA
#undef GX
      BAR_L();
      stamp(k, sb + 18);
      f32x4 fin[3];
      kexchange(k, acc, fin, 24 * SLOT, lane);
      v2u pk[3];
#pragma unroll
      for (int j = 0; j < 3; ++j) { pk[j] = pk4(x2r[j] + rnd4(x1r[j] + rnd4(fin[j]))); xres[j] = unpk(pk[j]); }
      if (ly == 0) {
        kstore(k, ws, WS_ACT + A_XO, pk, lane);
        kstats(k, ws, 2, xres, lane);
        BAR_VL0();
        post_flag(k, fl_group(PH_XO, r, c), tag_of(k, PH_XO));
      } else {
        unsigned char* po = opqp(out) + (size_t)(r * 64 + (w & 3) * 16 + t) * 1280 + (80 * c + 48 * kh + 4 * g) * 2;
#pragma unroll
        for (int j = 0; j < 3; ++j)
          if (!(j == 2 && kh)) *(GAS v2u*)(po + 32 * j) = pk[j];
      }
      stamp(k, sb + 19);
    }
    if (STOP_PHASE == layer * 7 + 5) return;
  }
  stampc(k, 127); stamp(k, 125); stamp_dump(k);
  if (k.b == 0 && tid == 0) __hip_atomic_store((GAS u64*)(wsp + WS_EPOCH), k.tag + 64, __ATOMIC_RELAXED, __HIP_MEMORY_SCOPE_AGENT);
}

extern "C" void kernel_launch(void* const* d_in, const int* in_sizes, int n_in, void* d_out, int out_size, void* d_ws, size_t ws_size,
                              hipStream_t stream) {
  (void)in_sizes; (void)n_in; (void)out_size; (void)ws_size;
  (void)hipFuncSetAttribute((const void*)hedgehog_smlp_kernel, hipFuncAttributeMaxDynamicSharedMemorySize, LDS_BYTES);
  hedgehog_smlp_kernel<<<256, 512, LDS_BYTES, stream>>>(
      (const unsigned char*)d_in[0], (const unsigned char*)d_in[1], (const unsigned char*)d_in[2], (const unsigned char*)d_in[3],
      (const unsigned char*)d_in[5], (const unsigned char*)d_in[6], (const unsigned char*)d_in[7], (const unsigned char*)d_in[8],
      (unsigned char*)d_out, (unsigned char*)d_ws);
}
```

```cpp
#include <hip/hip_runtime.h>
#include <hip/hip_bf16.h>
#include <stdint.h>

#define DI __device__ __forceinline__
#define LAS __attribute__((address_space(3)))
#define GAS __attribute__((address_space(1)))
typedef unsigned v4u __attribute__((ext_vector_type(4)));
typedef unsigned v2u __attribute__((ext_vector_type(2)));
typedef float f32x4 __attribute__((ext_vector_type(4)));
typedef float f32x2 __attribute__((ext_vector_type(2)));
typedef short bf16x8 __attribute__((ext_vector_type(8)));
typedef short s16x4 __attribute__((ext_vector_type(4)));
typedef __bf16 bf2_t __attribute__((ext_vector_type(2)));
typedef __amdgpu_buffer_rsrc_t srd_t;
typedef unsigned long long u64;

#define STOP_PHASE 1000
#define STAMPS 0
#define FINE 0
#define LATEW(w) ((w) == 7)
#define FST(id) { if (FINE) stamp(k, sb + (id)); }

constexpr int S_ = 2048, D_ = 640, F_ = 1536;
constexpr unsigned WS_EPOCH = 0, WS_TMO = 64;
constexpr unsigned WS_FLAGS = 4096;
constexpr unsigned WS_CS = 65536;
constexpr unsigned WS_STAMP = 131072;
constexpr unsigned WS_RED = 1u << 20;
constexpr unsigned WS_STAT = WS_RED + (1u << 19);
constexpr unsigned STAT_SZ = 128 * 2048;
constexpr unsigned WS_PART = 4u << 20;
constexpr unsigned PART_L = 256u * 16384u;
constexpr unsigned ACT20 = 128u * 20u * 1024u, ACT48 = 128u * 48u * 1024u;
constexpr unsigned WS_ACT = 16u << 20;
constexpr unsigned ACT_L = 5u * ACT20 + ACT48;
constexpr unsigned A_O = 0, A_X2 = ACT20, A_HS = 2 * ACT20, A_X1 = 3 * ACT20, A_XO = 4 * ACT20, A_H2 = 5 * ACT20;
constexpr unsigned WS_WT = 64u << 20;
constexpr unsigned WT_A = 0, WT_O = 327680, WT_1 = WT_O + 102400, WT_2 = WT_1 + 102400, WT_4 = WT_2 + 102400, WT_3 = WT_4 + 245760,
                   WT_C = WT_3 + 245760;
constexpr unsigned WS_END = WS_WT + 8 * WT_C;
enum { PH_KV1 = 0, PH_KV2, PH_O, PH_X2, PH_H, PH_X1, PH_H2, PH_XO, PH_PER_LAYER, PH_CS = 16, PH_T1 = 17, PH_T2 = 18 };
constexpr unsigned SLOT = 5120, NSLOT = 31, RINGP = 155;
constexpr unsigned L_MISC = SLOT * NSLOT;
constexpr unsigned L_STATS = L_MISC;
constexpr unsigned L_CSR = L_MISC + 512;
constexpr unsigned LDS_BYTES = 163840;
constexpr unsigned L_X = 0, L_XF = 76800;
constexpr unsigned L_IMG = 0, IMG_H = 10240;
constexpr unsigned L_S0 = 81920, L_SJ = 97280, L_SCS = 61440;
constexpr int P_C = 8, P_D = 28, P_E = 17, P_G = 6;
constexpr unsigned OOB = 0x80000000u;

#define WAIT_VM(n) asm volatile("s_waitcnt vmcnt(" #n ")" ::: "memory")
#define BAR() asm volatile("s_barrier" ::: "memory")
#define BAR_L() asm volatile("s_waitcnt lgkmcnt(0)\n\ts_barrier" ::: "memory")
#define BAR_VL0() asm volatile("s_waitcnt vmcnt(0) lgkmcnt(0)\n\ts_barrier" ::: "memory")
#define SB() __builtin_amdgcn_sched_barrier(0)
template <int N> DI void bar_vl() { asm volatile("s_waitcnt vmcnt(%0) lgkmcnt(0)\n\ts_barrier" ::"i"(N) : "memory"); }

DI void glds(unsigned lds, unsigned voff, srd_t srd, unsigned soff) {
  asm volatile("s_mov_b32 m0, %0\n\ts_nop 0\n\tbuffer_load_dwordx4 %1, %2, %3 offen lds" ::"s"(lds), "v"(voff), "s"(srd), "s"(soff) : "memory");
}
template <int N> DI void gln(unsigned lds, unsigned voff, srd_t srd, unsigned soff);
template <> DI void gln<1>(unsigned lds, unsigned voff, srd_t srd, unsigned soff) {
  asm volatile("s_mov_b32 m0, %0\n\ts_nop 0\n\tbuffer_load_dwordx4 %1, %2, %3 offen sc1 lds" ::"s"(lds), "v"(voff), "s"(srd), "s"(soff) : "memory");
}
template <> DI void gln<2>(unsigned lds, unsigned voff, srd_t srd, unsigned soff) {
  asm volatile("s_mov_b32 m0, %0\n\ts_nop 0\n\tbuffer_load_dwordx4 %1, %2, %3 offen sc1 lds\n\t"
               "buffer_load_dwordx4 %1, %2, %3 offen offset:1024 sc1 lds" ::"s"(lds), "v"(voff), "s"(srd), "s"(soff) : "memory");
}
template <> DI void gln<3>(unsigned lds, unsigned voff, srd_t srd, unsigned soff) {
  asm volatile("s_mov_b32 m0, %0\n\ts_nop 0\n\tbuffer_load_dwordx4 %1, %2, %3 offen sc1 lds\n\t"
               "buffer_load_dwordx4 %1, %2, %3 offen offset:1024 sc1 lds\n\tbuffer_load_dwordx4 %1, %2, %3 offen offset:2048 sc1 lds"
               ::"s"(lds), "v"(voff), "s"(srd), "s"(soff) : "memory");
}
template <> DI void gln<4>(unsigned lds, unsigned voff, srd_t srd, unsigned soff) {
  asm volatile("s_mov_b32 m0, %0\n\ts_nop 0\n\tbuffer_load_dwordx4 %1, %2, %3 offen sc1 lds\n\t"
               "buffer_load_dwordx4 %1, %2, %3 offen offset:1024 sc1 lds\n\tbuffer_load_dwordx4 %1, %2, %3 offen offset:2048 sc1 lds\n\t"
               "buffer_load_dwordx4 %1, %2, %3 offen offset:3072 sc1 lds" ::"s"(lds), "v"(voff), "s"(srd), "s"(soff) : "memory");
}
template <> DI void gln<6>(unsigned lds, unsigned voff, srd_t srd, unsigned soff);
template <> DI void gln<5>(unsigned lds, unsigned voff, srd_t srd, unsigned soff) { gln<4>(lds, voff, srd, soff); gln<1>(lds + 4096, voff, srd, soff + 4096); }
template <> DI void gln<6>(unsigned lds, unsigned voff, srd_t srd, unsigned soff) { gln<4>(lds, voff, srd, soff); gln<2>(lds + 4096, voff, srd, soff + 4096); }
template <int P> DI void gl1(unsigned lds, unsigned voff, srd_t srd, unsigned soff) {
  asm volatile("s_mov_b32 m0, %0\n\ts_nop 0\n\tbuffer_load_dwordx4 %1, %2, %3 offen offset:%4 sc1 lds"
               ::"s"(lds + (P >> 2) * 4096u), "v"(voff), "s"(srd), "s"(soff + (P >> 2) * 4096u), "i"((P & 3) * 1024) : "memory");
}
template <int OFF> DI void ld128(v4u& d, unsigned voff, srd_t srd, unsigned soff) {
  asm volatile("buffer_load_dwordx4 %0, %1, %2, %3 offen offset:%4 sc1" : "=v"(d) : "v"(voff), "s"(srd), "s"(soff), "i"(OFF) : "memory");
}
template <int OFF> DI void ld64(v2u& d, unsigned voff, srd_t srd, unsigned soff) {
  asm volatile("buffer_load_dwordx2 %0, %1, %2, %3 offen offset:%4 sc1" : "=v"(d) : "v"(voff), "s"(srd), "s"(soff), "i"(OFF) : "memory");
}
template <int N> DI void wait_tie(v4u& a) { asm volatile("s_waitcnt vmcnt(%1)" : "+v"(a) : "i"(N)); }
template <int N> DI void wait_tie2(v4u& a, v4u& b) { asm volatile("s_waitcnt vmcnt(%2)" : "+v"(a), "+v"(b) : "i"(N)); }
template <int N> DI void wait_tie3(v4u& a, v4u& b, v4u& c) { asm volatile("s_waitcnt vmcnt(%3)" : "+v"(a), "+v"(b), "+v"(c) : "i"(N)); }
DI void tie2(v4u& a, v4u& b) { asm volatile("" : "+v"(a), "+v"(b)); }
DI void tie3(v4u& a, v4u& b, v4u& c) { asm volatile("" : "+v"(a), "+v"(b), "+v"(c)); }
DI void tie1(v4u& a) { asm volatile("" : "+v"(a)); }
DI void st128(v4u v, unsigned voff, srd_t srd, unsigned soff) { __builtin_amdgcn_raw_buffer_store_b128(v, srd, voff, soff, 16); }
DI void st64(v2u v, unsigned voff, srd_t srd, unsigned soff) { __builtin_amdgcn_raw_buffer_store_b64(v, srd, voff, soff, 16); }

DI float asf(unsigned u) { return __builtin_bit_cast(float, u); }
DI unsigned asu(float f) { return __builtin_bit_cast(unsigned, f); }
DI srd_t mk_srd(const void* p, unsigned bytes) { return __builtin_amdgcn_make_buffer_rsrc((void*)p, 0, bytes, 0x00020000); }
DI f32x4 mfma(v4u a, v4u b, f32x4 c) {
  return __builtin_amdgcn_mfma_f32_16x16x32_bf16(__builtin_bit_cast(bf16x8, a), __builtin_bit_cast(bf16x8, b), c, 0, 0, 0);
}
DI unsigned pk2(float a, float b) { f32x2 v = {a, b}; return __builtin_bit_cast(unsigned, __builtin_convertvector(v, bf2_t)); }
DI unsigned pk2v(f32x2 v) { return __builtin_bit_cast(unsigned, __builtin_convertvector(v, bf2_t)); }
DI float bflo(unsigned u) { return asf(u << 16); }
DI float bfhi(unsigned u) { return asf(u & 0xFFFF0000u); }
DI f32x4 unpk(v2u u) { return f32x4{bflo(u.x), bfhi(u.x), bflo(u.y), bfhi(u.y)}; }
DI v2u pk4(f32x4 v) { return v2u{pk2(v.x, v.y), pk2(v.z, v.w)}; }
DI f32x4 rnd4(f32x4 v) { return unpk(pk4(v)); }
template <int ROW8> DI v4u trfrag(LAS unsigned char* p) {
  s16x4 lo = __builtin_amdgcn_ds_read_tr16_b64_v4i16((LAS s16x4*)p);
  s16x4 hi = __builtin_amdgcn_ds_read_tr16_b64_v4i16((LAS s16x4*)(p + ROW8));
  return __builtin_bit_cast(v4u, __builtin_shufflevector(lo, hi, 0, 1, 2, 3, 4, 5, 6, 7));
}
DI int tr_row(int lane) { const int g = lane >> 4; return 16 * (g >> 1) + 4 * (g & 1) + ((lane >> 2) & 3); }
DI void xr_sum2(float& a, float& b) {
  auto s = __builtin_amdgcn_permlane16_swap(asu(a), asu(b), false, false);
  const float m = asf(s[0]) + asf(s[1]);
  auto t = __builtin_amdgcn_permlane32_swap(asu(m), asu(m), false, false);
  const float n = asf(t[0]) + asf(t[1]);
  auto u = __builtin_amdgcn_permlane16_swap(asu(n), asu(n), false, false);
  a = asf(u[0]); b = asf(u[1]);
}
DI float fmax_raw(float a, float b) { float r; asm("v_max_f32 %0, %1, %2" : "=v"(r) : "v"(a), "v"(b)); return r; }
DI float fmax3_raw(float a, float b, float c) { float r; asm("v_max3_f32 %0, %1, %2, %3" : "=v"(r) : "v"(a), "v"(b), "v"(c)); return r; }
DI float fmin3_raw(float a, float b, float c) { float r; asm("v_min3_f32 %0, %1, %2, %3" : "=v"(r) : "v"(a), "v"(b), "v"(c)); return r; }
DI void xr_max2(float& a, float& b) {
  auto s = __builtin_amdgcn_permlane16_swap(asu(a), asu(b), false, false);
  const float m = __builtin_fmaxf(asf(s[0]), asf(s[1]));
  auto t = __builtin_amdgcn_permlane32_swap(asu(m), asu(m), false, false);
  const float n = __builtin_fmaxf(asf(t[0]), asf(t[1]));
  auto u = __builtin_amdgcn_permlane16_swap(asu(n), asu(n), false, false);
  a = asf(u[0]); b = asf(u[1]);
}
template <bool MAX> DI void xr4(float (&a)[4], float (&b)[4]) {
  float m[4], n[4];
#pragma unroll
  for (int i = 0; i < 4; ++i) { auto s = __builtin_amdgcn_permlane16_swap(asu(a[i]), asu(b[i]), false, false);
                                m[i] = MAX ? fmax_raw(asf(s[0]), asf(s[1])) : asf(s[0]) + asf(s[1]); }
#pragma unroll
  for (int i = 0; i < 4; ++i) { auto t = __builtin_amdgcn_permlane32_swap(asu(m[i]), asu(m[i]), false, false);
                                n[i] = MAX ? fmax_raw(asf(t[0]), asf(t[1])) : asf(t[0]) + asf(t[1]); }
#pragma unroll
  for (int i = 0; i < 4; ++i) { auto u = __builtin_amdgcn_permlane16_swap(asu(n[i]), asu(n[i]), false, false); a[i] = asf(u[0]); b[i] = asf(u[1]); }
}
DI f32x2 gelu2(f32x2 x) {
  constexpr float C0 = -2.0f * 0.7978845608028654f * 1.4426950408889634f, C1 = C0 * 0.044715f;
  const f32x2 a = x * ((x * x) * C1 + C0);
  const f32x2 d = f32x2{__builtin_amdgcn_exp2f(a.x), __builtin_amdgcn_exp2f(a.y)} + 1.0f;
  return x * f32x2{__builtin_amdgcn_rcpf(d.x), __builtin_amdgcn_rcpf(d.y)};
}
DI v2u ln_gelu4(f32x4 acc, f32x4 cs, float nmean, float scale) {
  const f32x2 a = (f32x2{cs.x, cs.y} * nmean + f32x2{acc.x, acc.y}) * scale, b = (f32x2{cs.z, cs.w} * nmean + f32x2{acc.z, acc.w}) * scale;
  return v2u{pk2v(gelu2(a)), pk2v(gelu2(b))};
}
DI int opq(int x) { asm volatile("" : "+v"(x)); return x; }
DI int opqs(int x) { asm volatile("" : "+s"(x)); return x; }
template <class T> DI T* opqp(T* p) { asm volatile("" : "+s"(p)); return p; }

struct Ctx {
  unsigned char* ws;
  LAS unsigned char* L;
  int w, r, c, b;
  u64 tag;
  bool dead;
  DI void fresh() { w = opqs(w); r = opqs(r); c = opqs(c); ws = opqp(ws); }
};

DI void stamp(const Ctx&, int) {}
DI void stampc(const Ctx&, int) {}
DI void stamp_dump(const Ctx&) {}
DI void stampw(const Ctx&, int) {}

DI void poll_flags(Ctx& k, unsigned flag_byte_off, u64 want) {
  if (k.w == 7 && !k.dead) {
    GAS u64* p = (GAS u64*)(k.ws + WS_FLAGS + flag_byte_off);
    unsigned spins = 0;
    for (;;) {
      const u64 v = __hip_atomic_load(p, __ATOMIC_RELAXED, __HIP_MEMORY_SCOPE_AGENT);
      if (__all(v == want)) break;
      if (++spins > (1u << 20)) { k.dead = true; __hip_atomic_store((GAS unsigned*)(k.ws + WS_TMO), 1u, __ATOMIC_RELAXED, __HIP_MEMORY_SCOPE_AGENT); break; }
    }
  }
}
DI void poll_flags_any(Ctx& k, unsigned flag_byte_off, u64 want) {
  if (!k.dead) {
    GAS u64* p = (GAS u64*)(k.ws + WS_FLAGS + flag_byte_off);
    unsigned spins = 0;
    for (;;) {
      const u64 v = __hip_atomic_load(p, __ATOMIC_RELAXED, __HIP_MEMORY_SCOPE_AGENT);
      if (__all(v == want)) break;
      if (++spins > (1u << 20)) { k.dead = true; __hip_atomic_store((GAS unsigned*)(k.ws + WS_TMO), 1u, __ATOMIC_RELAXED, __HIP_MEMORY_SCOPE_AGENT); break; }
    }
  }
}
DI v4u granule(f32x4 v, u64 tg) { return v4u{pk2(v.x, v.y), (unsigned)tg, pk2(v.z, v.w), (unsigned)(tg >> 32)}; }
DI bool granule_ok(v4u g, u64 tg) { return g.y == (unsigned)tg && g.w == (unsigned)(tg >> 32); }
DI bool spin_more(Ctx& k, unsigned& spins) {
  if (k.dead) return false;
  __builtin_amdgcn_s_sleep(2);
  if (++spins > (1u << 19)) { k.dead = true; __hip_atomic_store((GAS unsigned*)(k.ws + WS_TMO), 1u, __ATOMIC_RELAXED, __HIP_MEMORY_SCOPE_AGENT); return false; }
  return true;
}
DI void post_flag(const Ctx& k, unsigned flag_byte_off, u64 v) {
  if (threadIdx.x == 7 * 64) __hip_atomic_store((GAS u64*)(k.ws + WS_FLAGS + flag_byte_off), v, __ATOMIC_RELAXED, __HIP_MEMORY_SCOPE_AGENT);
}
DI u64 tag_of(const Ctx& k, int ph) { return k.tag + (u64)(ph + 1); }
DI unsigned fl_group(int ph, int r, int c) { return (unsigned)((ph * 256 + r * 8 + c) * 8); }
DI unsigned fl_col(int ph, int r, int c) { return (unsigned)((ph * 256 + c * 32 + r) * 8); }

template <int NW> DI void wstream(const Ctx& k, srd_t ws, unsigned src, int lp, int np, unsigned v16) {
  if (k.w < NW) {
#pragma unroll 1
    for (int g = k.w * 4; g < np; g += NW * 4) {
      const int s = g + 4 > np ? np - 4 : g;
      gln<4>((unsigned)(lp + s) * 1024u, v16, ws, src + (unsigned)s * 1024u);
    }
  }
}
template <int G> DI void wstream_exact(const Ctx& k, srd_t ws, unsigned src, int lp, int np, unsigned v16) {
  const int g = k.w * G, s = g + G > np ? np - G : g;
  gln<G>((unsigned)(lp + s) * 1024u, v16, ws, src + (unsigned)s * 1024u);
}

struct StatLd { v2u s0, s1, s2, s3; };
DI void stat_issue(StatLd& s, srd_t ws, unsigned statbuf, int tb, int lane) {
  const unsigned vo = (unsigned)((lane >> 4) * 512 + (lane & 15) * 8);
  const unsigned so = WS_STAT + statbuf * STAT_SZ + (unsigned)tb * 2048u;
  ld64<0>(s.s0, vo, ws, so); ld64<128>(s.s1, vo, ws, so); ld64<256>(s.s2, vo, ws, so); ld64<384>(s.s3, vo, ws, so);
}
DI void stat_finish(const StatLd& s, float& mean, float& var) {
  float a = (asf(s.s0.x) + asf(s.s1.x)) + (asf(s.s2.x) + asf(s.s3.x));
  float q = (asf(s.s0.y) + asf(s.s1.y)) + (asf(s.s2.y) + asf(s.s3.y));
  xr_sum2(a, q);
  mean = a * (1.0f / 640.0f);
  var = __builtin_fmaxf(__builtin_fmaf(-mean, mean, q * (1.0f / 640.0f)), 0.0f);
}
template <int N> DI void wait_tie_stat(StatLd& s) { asm volatile("s_waitcnt vmcnt(%4)" : "+v"(s.s0), "+v"(s.s1), "+v"(s.s2), "+v"(s.s3) : "i"(N)); }

#define MIX(NM, ND) { _Pragma("unroll") for (int m_ = 0; m_ < ND; ++m_) { __builtin_amdgcn_sched_group_barrier(0x008, NM, 0); __builtin_amdgcn_sched_group_barrier(0x100, 1, 0); } }
struct RingAddr { LAS unsigned char *b0, *b1; };
DI RingAddr ring_addr(const Ctx& k, int lane) {
  const int kh = k.w >> 2;
  return RingAddr{k.L + lane * 16 + kh * SLOT, k.L + lane * 16 + (kh ? 0 : 30 * SLOT)};
}
template <int N0> DI void ring_read(const RingAddr& a, v4u (&d)[5]) {
  constexpr int sA = N0 % 31;
  LAS unsigned char* p = sA == 30 ? a.b1 : a.b0 + sA * SLOT;
#pragma unroll
  for (int bl = 0; bl < 5; ++bl) d[bl] = *(LAS v4u*)(p + 1024 * bl);
}
#define KMAIN(P, NS, NL, PF)                                                                                           \
  {                                                                                                                    \
    v4u wf[2][5];                                                                                                      \
    ring_read<P>(ra, wf[0]);                                                                                           \
    KSTEP(P, NS, NL, PF, 0) KSTEP(P, NS, NL, PF, 1) KSTEP(P, NS, NL, PF, 2) KSTEP(P, NS, NL, PF, 3) KSTEP(P, NS, NL, PF, 4) \
    KSTEP(P, NS, NL, PF, 5) KSTEP(P, NS, NL, PF, 6) KSTEP(P, NS, NL, PF, 7) KSTEP(P, NS, NL, PF, 8) KSTEP(P, NS, NL, PF, 9) \
  }
#define KSTEP(P, NS, NL, PF, i)                                                               \
  {                                                                                           \
    wait_tie<9 - i + NS + (i < 1 ? 0 : (i - 1 < NL ? i - 1 : NL))>(xf[i]);                    \
    if (i + 1 < 10) ring_read<P + 2 * (i + 1)>(ra, wf[(i + 1) & 1]);                          \
    _Pragma("unroll") for (int bl = 0; bl < 5; ++bl) acc[bl] = mfma(wf[i & 1][bl], xf[i], acc[bl]); \
    if (i + 1 < 10) MIX(1, 5)                                                                 \
    SB();                                                                                     \
    if (i >= 1 && i <= NL) PF((i >= 1 ? i - 1 : 0))                                           \
  }
DI unsigned pin(unsigned x) { asm volatile("" : "+s"(x)); return x; }
DI unsigned pinv(unsigned x) { asm volatile("" : "+v"(x)); return x; }
DI unsigned kx_off(const Ctx& k, unsigned actbuf) { return pin(actbuf + (unsigned)k.r * 81920u + (unsigned)(k.w >> 2) * 40960u + (unsigned)(k.w & 3) * 1024u); }
DI void kx_issue(v4u (&x)[10], srd_t ws, unsigned so, unsigned vo) {
#pragma unroll
  for (int i = 0; i < 10; ++i) ld128<0>(x[i], vo, ws, so + i * 4096u);
}
DI void kexchange(const Ctx& k, f32x4 (&acc)[5], f32x4 (&fin)[3], unsigned scratch, int lane) {
  LAS f32x4* sc = (LAS f32x4*)(k.L + scratch + (unsigned)(k.w & 3) * 5120u + lane * 16);
  if ((k.w >> 2) == 0) { sc[3 * 64] = acc[3]; sc[4 * 64] = acc[4]; fin[0] = acc[0]; fin[1] = acc[1]; fin[2] = acc[2]; }
  else { sc[0] = acc[0]; sc[64] = acc[1]; sc[128] = acc[2]; fin[0] = acc[3]; fin[1] = acc[4]; fin[2] = f32x4{0.f, 0.f, 0.f, 0.f}; }
  BAR_L();
  const int fb = (k.w >> 2) * 3;
  fin[0] += sc[fb * 64]; fin[1] += sc[(fb + 1) * 64];
  if (fb == 0) fin[2] += sc[128];
}
DI unsigned frag_off(int B, int gq, int t) { return (unsigned)((B >> 1) * 4096 + (2 * (B & 1) + (gq & 1)) * 256 + t * 16 + (gq >> 1) * 8); }
DI unsigned blk_off(int B) { return (unsigned)((B >> 1) * 4096 + (B & 1) * 512); }
DI void kstore(const Ctx& k, srd_t ws, unsigned actbuf, const v2u (&pk)[3], int lane) {
  const int kh = k.w >> 2;
  const unsigned lo = frag_off(0, lane >> 4, lane & 15);
  const unsigned so = actbuf + (unsigned)k.r * 81920u + (unsigned)(k.w & 3) * 1024u;
#pragma unroll
  for (int j = 0; j < 3; ++j) {
    const int B = 5 * k.c + 3 * kh + j;
    if (!(j == 2 && kh)) st64(pk[j], lo, ws, so + blk_off(B));
  }
}
DI void kstats(const Ctx& k, srd_t ws, unsigned statbuf, const f32x4 (&v)[3], int lane) {
  const int kh = k.w >> 2;
  f32x2 s = f32x2{v[0].x, v[0].y} + f32x2{v[0].z, v[0].w} + f32x2{v[1].x, v[1].y} + f32x2{v[1].z, v[1].w};
  f32x2 q = f32x2{v[0].x, v[0].y} * f32x2{v[0].x, v[0].y} + f32x2{v[0].z, v[0].w} * f32x2{v[0].z, v[0].w};
  q = f32x2{v[1].x, v[1].y} * f32x2{v[1].x, v[1].y} + q; q = f32x2{v[1].z, v[1].w} * f32x2{v[1].z, v[1].w} + q;
  if (kh == 0) {
    s += f32x2{v[2].x, v[2].y} + f32x2{v[2].z, v[2].w};
    q = f32x2{v[2].x, v[2].y} * f32x2{v[2].x, v[2].y} + q; q = f32x2{v[2].z, v[2].w} * f32x2{v[2].z, v[2].w} + q;
  }
  float s1 = s.x + s.y, q1 = q.x + q.y;
  xr_sum2(s1, q1);
  const unsigned vo = lane < 16 ? (unsigned)(lane * 8) : OOB;
  st64(v2u{asu(s1), asu(q1)}, vo, ws, WS_STAT + statbuf * STAT_SZ + (unsigned)((k.r * 4 + (k.w & 3)) * 2048 + (2 * k.c + kh) * 128));
}
constexpr int g_xwait(int s) {
  constexpr int t[24] = {63, 8, 8, 8, 8, 10, 11, 12, 13, 15, 16, 17, 18, 18, 16, 15, 14, 12, 9, 7, 63, 63, 63, 63};
  return t[s];
}

__global__ void __launch_bounds__(512, 2)
hedgehog_smlp_kernel(const unsigned char* x, const unsigned char* wqkv, const unsigned char* wphi, const unsigned char* wo,
                     const unsigned char* w1, const unsigned char* w2, const unsigned char* w3, const unsigned char* w4,
                     unsigned char* out, unsigned char* wsp) {
  extern __shared__ __attribute__((aligned(16))) unsigned char smem[];
  Ctx k;
  k.ws = wsp;
  k.L = (LAS unsigned char*)smem;
  const int tid = threadIdx.x;
  const int lane0 = tid & 63;
  k.w = __builtin_amdgcn_readfirstlane(tid >> 6);
  k.b = blockIdx.x;
#define MAPMODE 2
  { const int xc = k.b & 7, i = k.b >> 3;
    if (MAPMODE == 0) { k.r = xc * 4 + (i >> 3); k.c = i & 7; }
    else if (MAPMODE == 2) { k.r = (xc >> 1) * 8 + (i >> 2); k.c = (xc & 1) * 4 + (i & 3); }
    else { k.r = (xc >> 2) * 16 + (i >> 1); k.c = (xc & 3) * 2 + (i & 1); } }
  k.dead = false;
  f32x4 xres[3];
  v4u aphi[2];
#define PHASE_VARS()                                                             \
  k.fresh();                                                                     \
  const int w = k.w, r = k.r, c = k.c;                                           \
  const srd_t ws = mk_srd(k.ws, WS_END);                                         \
  const int ly = opqs(layer), ph0 = ly * PH_PER_LAYER, sb = 2 + ly * 64;         \
  const unsigned act = WS_ACT + (unsigned)ly * ACT_L;                            \
  const unsigned wt = WS_WT + (unsigned)c * WT_C;                                \
  (void)w; (void)r; (void)c; (void)ph0; (void)sb; (void)act; (void)wt;
  stamp(k, 0); stampc(k, 126);
#define CODEPF 1
  constexpr int CODE_KB = 38;
  v4u code_sink = {0u, 0u, 0u, 0u};
  if (CODEPF) {
    unsigned long long pc;
    asm volatile("s_getpc_b64 %0" : "=s"(pc));
    const unsigned long long a = pc + (unsigned long long)((((k.b >> 3) * 8 + k.w) % CODE_KB) * 1024 + lane0 * 16);
    asm volatile("global_load_dwordx4 %0, %1, off" : "=v"(code_sink) : "v"(a) : "memory");
  }
  {
    const int w = k.w, r = k.r, c = k.c;
    const srd_t ws = mk_srd(wsp, WS_END);
    const unsigned wt = WS_WT + (unsigned)c * WT_C;
    const int lane = opq(lane0), g = lane >> 4, p = lane & 3, trr = tr_row(lane);
    if (r < 20) {
      const srd_t srd = mk_srd(wqkv + 160 * c + (size_t)r * 122880, 122880 - 160 * c);
#pragma unroll
      for (int u = 0; u < 2; ++u) {
        const unsigned id = (2 * w + u) * 64 + lane, row = (id * 2185u) >> 16, rem = id - row * 30u, sec = (rem * 6554u) >> 16;
        if (2 * w + u < 15) glds(L_S0 + (2 * w + u) * 1024u, row * 3840u + sec * 1280u + (rem - sec * 10u) * 16u, srd, 0);
      }
    }
    const int J = r >= 20 ? (r - 20) * 8 + w : 96 + r * 8 + w;
    const unsigned sj = L_SJ + w * 6144u;
    if (J < 108) {
      const unsigned char* W = J < 20 ? wo : J < 40 ? w1 : J < 60 ? w2 : w4;
      const int s = J < 20 ? J : J < 40 ? J - 20 : J < 60 ? J - 40 : J - 60;
      const srd_t srd = mk_srd(W + 160 * c + (size_t)s * 40960, 40960 - 160 * c);
#pragma unroll
      for (int j = 0; j < 5; ++j) {
        const unsigned t = 64 * j + lane, row = (t * 6554u) >> 16;
        glds(sj + j * 1024u, row * 1280u + (t - row * 10u) * 16u, srd, 0);
      }
    } else if (J < 148) {
      const int s = (J - 108) >> 1, half = (J - 108) & 1;
      const srd_t srd = mk_srd(w3 + 384 * c + 192 * half + (size_t)s * 98304, 98304 - 384 * c - 192 * half);
#pragma unroll
      for (int j = 0; j < 6; ++j) {
        const unsigned t = 64 * j + lane, row = (t * 5462u) >> 16;
        glds(sj + j * 1024u, row * 3072u + (t - row * 12u) * 16u, srd, 0);
      }
    }
    {
      const unsigned char* W; unsigned ld, col;
      if (k.b < 120) { W = wqkv; ld = 3840; col = k.b * 16; }
      else if (k.b < 160) { W = w1; ld = 1280; col = (k.b - 120) * 16; }
      else { W = w3; ld = 3072; col = (k.b - 160) * 16; }
      const unsigned vo = (unsigned)(lane >> 1) * ld + (lane & 1) * 16;
      const srd_t srd = mk_srd(W + col * 2, 640u * ld - col * 2);
#pragma unroll
      for (int u = 0; u < 3; ++u) {
        const int s = w + 8 * u;
        if (s < 20) glds(L_SCS + s * 1024u, vo, srd, s * 32u * ld);
      }
    }
    {
      u64 e = __hip_atomic_load((GAS u64*)(wsp + WS_EPOCH), __ATOMIC_RELAXED, __HIP_MEMORY_SCOPE_AGENT);
      const unsigned lo = __builtin_amdgcn_readfirstlane((unsigned)e), hi = __builtin_amdgcn_readfirstlane((unsigned)(e >> 32));
      k.tag = ((u64)hi << 32) | lo;
    }
    v4u xin0[10];
    {
      const GAS v4u* px = (const GAS v4u*)(x + (size_t)(r * 64 + (tid >> 3)) * 1280 + (tid & 7) * 16);
#pragma unroll
      for (int i = 0; i < 10; ++i) xin0[i] = px[8 * i];
    }
    {
      const int kh = w >> 2;
      const unsigned char* px = x + (size_t)(r * 64 + (w & 3) * 16 + (lane & 15)) * 1280 + (80 * c + 48 * kh + 4 * g) * 2;
#pragma unroll
      for (int j = 0; j < 3; ++j) {
        const v2u u = (j == 2 && kh) ? v2u{0u, 0u} : *(const GAS v2u*)(px + 32 * j);
        xres[j] = unpk(u);
      }
    }
    {
      const int row = lane & 15, h = 4 * c + (w >> 1);
      const GAS unsigned short* pw = (const GAS unsigned short*)wphi + h * 400;
#pragma unroll
      for (int fb = 0; fb < 2; ++fb) {
        const bool rowok = fb == 0 || (row & 3) == 0;
        const int f = 5 * (row >> 2) + (fb == 0 ? (row & 3) : 4);
        unsigned e[8];
#pragma unroll
        for (int j = 0; j < 8; ++j) {
          const bool ok = rowok && (j < 4 || g == 0);
          const int d = j < 4 ? 4 * g + j : 16 + (j - 4);
          const unsigned v = pw[(ok ? d : 0) * 20 + f];
          e[j] = ok ? v : 0u;
        }
        aphi[fb] = v4u{e[0] | (e[1] << 16), e[2] | (e[3] << 16), e[4] | (e[5] << 16), e[6] | (e[7] << 16)};
      }
    }
    BAR_VL0();
    asm volatile("" ::"v"(code_sink));
    stamp(k, 120);
    if (r < 20) {
      const int j = w >> 1, role = w & 1, vqp = role == 0 ? p : (p == 1 ? 0 : 4);
      const v4u f0 = trfrag<3840>(k.L + L_S0 + trr * 480 + role * 160 + 40 * j + 8 * p);
      const v4u f1 = trfrag<3840>(k.L + L_S0 + trr * 480 + 40 * j + (p == 0 ? role * 160 + 32 : 320 + 8 * vqp));
      st128(f0, lane * 16, ws, wt + WT_A + r * 16384u + w * 2048u);
      st128(f1, lane * 16, ws, wt + WT_A + r * 16384u + w * 2048u + 1024u);
      BAR_VL0();
      post_flag(k, fl_col(PH_T1, r, c), tag_of(k, PH_T1));
    }
    if (J < 108) {
      const int s = J < 20 ? J : J < 40 ? J - 20 : J < 60 ? J - 40 : J - 60;
      const int n = J < 60 ? 2 * (s % 10) + s / 10 : 2 * (s % 24) + s / 24;
      const unsigned dst = wt + (J < 20 ? WT_O : J < 40 ? WT_1 : J < 60 ? WT_2 : WT_4) + n * 5120u;
#pragma unroll
      for (int bl = 0; bl < 5; ++bl) st128(trfrag<1280>(k.L + sj + trr * 160 + p * 8 + 32 * bl), lane * 16, ws, dst + bl * 1024u);
    } else if (J < 148) {
      const int s = (J - 108) >> 1, half = (J - 108) & 1;
#pragma unroll
      for (int bl = 0; bl < 6; ++bl)
        st128(trfrag<1536>(k.L + sj + trr * 192 + p * 8 + 32 * bl), lane * 16, ws, wt + WT_3 + s * 12288u + (6 * half + bl) * 1024u);
    }
    {
      f32x4 cs = {0.f, 0.f, 0.f, 0.f};
      const v4u ones = {0x3F803F80u, 0x3F803F80u, 0x3F803F80u, 0x3F803F80u};
#pragma unroll
      for (int u = 0; u < 3; ++u) {
        const int s = w + 8 * u;
        if (s < 20) cs = mfma(trfrag<256>(k.L + L_SCS + s * 1024u + trr * 32 + p * 8), ones, cs);
      }
      if ((lane & 15) == 0) *(LAS f32x4*)(k.L + L_CSR + w * 64 + g * 16) = cs;
      BAR_L();
      if (w == 0) {
        if (lane < 16) {
          float s = 0.f;
#pragma unroll
          for (int u = 0; u < 8; ++u) s += *(LAS float*)(k.L + L_CSR + u * 64 + lane * 4);
          __hip_atomic_store((GAS float*)(wsp + WS_CS) + k.b * 16 + lane, s, __ATOMIC_RELAXED, __HIP_MEMORY_SCOPE_AGENT);
        }
        WAIT_VM(0);
        if (lane == 0) __hip_atomic_store((GAS u64*)(wsp + WS_FLAGS + PH_CS * 2048 + k.b * 8), tag_of(k, PH_CS), __ATOMIC_RELAXED, __HIP_MEMORY_SCOPE_AGENT);
      }
    }
    {
      const int tok = tid >> 3, part = tid & 7;
      float s = 0.f, q = 0.f;
#pragma unroll
      for (int i = 0; i < 10; ++i) {
        const int u = part + 8 * i;
        const int B = u >> 1, gq = (u & 1) * 2;
        LAS unsigned char* q0 = k.L + L_X + ((B >> 1) * 4 + (tok >> 4)) * 1024 + (tok & 15) * 16 + (B & 1) * 512 + (gq >> 1) * 8;
        *(LAS v2u*)(q0) = v2u{xin0[i].x, xin0[i].y};
        *(LAS v2u*)(q0 + 256) = v2u{xin0[i].z, xin0[i].w};
#pragma unroll
        for (int e = 0; e < 4; ++e) {
          const float a = bflo(xin0[i][e]), bb = bfhi(xin0[i][e]);
          s += a + bb; q = __builtin_fmaf(a, a, q); q = __builtin_fmaf(bb, bb, q);
        }
      }
      s += __shfl_xor(s, 1); q += __shfl_xor(q, 1); s += __shfl_xor(s, 2); q += __shfl_xor(q, 2); s += __shfl_xor(s, 4); q += __shfl_xor(q, 4);
      const float mean = s * (1.0f / 640.0f), var = __builtin_fmaxf(__builtin_fmaf(-mean, mean, q * (1.0f / 640.0f)), 0.0f);
      if (part == 0) *(LAS f32x2*)(k.L + L_STATS + tok * 8) = f32x2{mean, __builtin_amdgcn_rsqf(var + 1e-5f)};
    }
    BAR_VL0();
    post_flag(k, fl_col(PH_T2, r, c), tag_of(k, PH_T2));
  }
  stamp(k, 1);

  f32x4 x2r[3], x1r[3];

#pragma unroll 1
  for (int layer = 0; layer < 2; ++layer) {
    {
      PHASE_VARS()
      const int lane = opq(lane0), t = lane & 15, g = lane >> 4, j = w >> 1, role = w & 1;
      const unsigned v16 = pinv(lane * 16);
      const unsigned wa = pin(wt + WT_A + w * 2048u);
#define A_KS(i) (i)
#define A_WLOAD(i) { ld128<0>(wr[(i) % 5][0], v16, ws, wa + A_KS(i) * 16384u); ld128<1024>(wr[(i) % 5][1], v16, ws, wa + A_KS(i) * 16384u); }
#define A_WINDOW() { A_WLOAD(0) A_WLOAD(1) }
      const int hcol = (4 * c + j) * 20;
      const int vq = role == 0 ? g : (g == 1 ? 0 : 4);
      const unsigned col0 = pinv((role * 640 + hcol + 4 * g) * 4), col1 = pinv((g == 0 ? role * 640 + hcol + 16 : 1280 + hcol + 4 * vq) * 4);
      const unsigned xa = pin((unsigned)(w < 4 ? w : w + 6) * 4096u), xb2 = pin((unsigned)(w < 4 ? 8 + (w >> 1) : 16 + (w >> 1)) * 4096u + (w & 1) * 2048u);
      const unsigned xsrc = pin(WS_ACT + A_XO + (unsigned)r * 81920u);
      v4u wr[5][2];
#pragma unroll
      for (int i = 0; i < 5; ++i) asm volatile("" : "=v"(wr[i][0]), "=v"(wr[i][1]));
      if (FINE == 5 && ly == 1) stampw(k, 128);
      if (ly != 0 && w != 7) A_WINDOW()
      if (FINE == 5 && ly == 1) { WAIT_VM(0); stampw(k, 136); }
      if (ly == 0) poll_flags(k, fl_col(PH_T1, (lane & 31) < 20 ? (lane & 31) : 0, c), tag_of(k, PH_T1));
      else poll_flags(k, fl_group(PH_XO, r, lane & 7), tag_of(k, PH_XO));
      BAR();
      if (ly == 0) {
        A_WINDOW()
        const int f = lane < 15 ? (lane / 5) * 40 + 5 * c + lane % 5 : lane < 20 ? 120 + 5 * c + (lane - 15) : 160 + 12 * c + ((lane - 20) % 12);
        poll_flags(k, PH_CS * 2048 + f * 8, tag_of(k, PH_CS));
        poll_flags(k, fl_col(PH_T2, lane & 31, c), tag_of(k, PH_T2));
        BAR_VL0();
      } else {
        FST(42)
        gln<4>(L_X + xa, v16, ws, xsrc + xa); gln<4>(L_X + xa + 16384u, v16, ws, xsrc + xa + 16384u); gln<2>(L_X + xb2, v16, ws, xsrc + xb2);
        if (w == 7) A_WINDOW()
        if (FINE == 5) { WAIT_VM(0); stampw(k, 144); }
        if (w < 4) WAIT_VM(6); else if (w < 7) WAIT_VM(10);
        BAR();
      }
      v4u cs0, cs1;
      StatLd st;
      A_WLOAD(2) A_WLOAD(3) A_WLOAD(4)
      stamp(k, sb + 0);
      f32x4 acc[2][4];
#pragma unroll
      for (int bl = 0; bl < 2; ++bl)
#pragma unroll
        for (int tb = 0; tb < 4; ++tb) acc[bl][tb] = f32x4{0.f, 0.f, 0.f, 0.f};
      v4u xf[2][4];
      LAS unsigned char* xb = k.L + L_X + v16;
#define A_READ(i) { _Pragma("unroll") for (int tb = 0; tb < 4; ++tb) xf[(i) & 1][tb] = *(LAS v4u*)(xb + ((i) * 4 + tb) * 1024); }
#define A_IT(i, N)                                                                                    \
  {                                                                                                   \
    wait_tie2<N>(wr[(i) % 5][0], wr[(i) % 5][1]);                                                     \
    if ((i) == 3) BAR();                    \
    if ((i) + 1 < 20) A_READ((i) + 1)                                                                 \
    _Pragma("unroll") for (int tb = 0; tb < 4; ++tb) {                                                \
      acc[0][tb] = mfma(wr[(i) % 5][0], xf[(i) & 1][tb], acc[0][tb]);                                 \
      acc[1][tb] = mfma(wr[(i) % 5][1], xf[(i) & 1][tb], acc[1][tb]);                                 \
    }                                                                                                 \
    if ((i) + 1 < 20) MIX(2, 4)                                                                       \
    SB();                                                                                             \
    if ((i) + 5 < 20) A_WLOAD((i) + 5)                                                                \
    if ((i) == 1) { stat_issue(st, ws, 2, r * 4 + (w & 3), lane);        \
                    ld128<0>(cs0, col0, ws, WS_CS); ld128<0>(cs1, col1, ws, WS_CS); }                 \
  }
      FST(20)
      A_READ(0)
#define A_W7() if (w == 7) WAIT_VM(8);
      A_W7() A_IT(0, 63) A_W7() A_IT(1, 63) A_IT(2, 14) A_IT(3, 14) FST(21) A_IT(4, 14) A_IT(5, 14) A_IT(6, 14) A_IT(7, 8) FST(22)
      A_IT(8, 8) A_IT(9, 8) A_IT(10, 8) A_IT(11, 8) FST(23) A_IT(12, 8) A_IT(13, 8) A_IT(14, 8) A_IT(15, 8) FST(24)
      A_IT(16, 6) A_IT(17, 4) A_IT(18, 2) A_IT(19, 0)
#undef A_IT
#undef A_W7
#undef A_READ
#undef A_WINDOW
#undef A_WLOAD
#undef A_KS
      if (ly) {
        wait_tie_stat<0>(st);
        float mean, var; stat_finish(st, mean, var);
        if (lane < 16) *(LAS f32x2*)(k.L + L_STATS + ((w & 3) * 16 + lane) * 8) = f32x2{mean, __builtin_amdgcn_rsqf(var + 1e-5f)};
      }
      BAR_L();
      stamp(k, sb + 1);
      tie2(cs0, cs1);
      const f32x4 c0 = __builtin_bit_cast(f32x4, cs0), c1 = __builtin_bit_cast(f32x4, cs1);
      unsigned phq[4][5];
      LAS unsigned char* img = k.L + L_IMG + j * IMG_H;
      {
        v4u bfr[4];
        f32x2 ms[4];
#pragma unroll
        for (int tb = 0; tb < 4; ++tb) ms[tb] = *(LAS f32x2*)(k.L + L_STATS + (tb * 16 + t) * 8);
#pragma unroll
        for (int tb = 0; tb < 4; ++tb) {
          const float nm = -ms[tb].x, rs = ms[tb].y;
          const f32x4 a0 = acc[0][tb], a1 = acc[1][tb];
          bfr[tb] = v4u{pk2v((f32x2{c0.x, c0.y} * nm + f32x2{a0.x, a0.y}) * rs), pk2v((f32x2{c0.z, c0.w} * nm + f32x2{a0.z, a0.w}) * rs),
                        pk2v((f32x2{c1.x, c1.y} * nm + f32x2{a1.x, a1.y}) * rs), pk2v((f32x2{c1.z, c1.w} * nm + f32x2{a1.z, a1.w}) * rs)};
        }
#define WO_GROUP(u) { const int g_ = (u) * 8 + w; if (g_ < 25) gln<4>((unsigned)(P_C * 5 + 4 * g_) * 1024u, v16, ws, wt + WT_O + (unsigned)g_ * 4096u); }
        WO_GROUP(0)
        f32x4 pa[4], pb[4];
        const f32x4 z = {0.f, 0.f, 0.f, 0.f};
#pragma unroll
        for (int tb = 0; tb < 4; ++tb) { pa[tb] = mfma(aphi[0], bfr[tb], z); pb[tb] = mfma(aphi[1], bfr[tb], z); }
        {
          const int vslot = role == 0 ? (g == 0 ? 6 : g) : (g == 0 ? 7 : g == 1 ? 0 : g == 2 ? 4 : 5);
          const bool ones = role == 1 && g == 3;
          LAS unsigned char* vrow = img + t * 160 + 96 + 8 * vslot;
#pragma unroll
          for (int tb = 0; tb < 4; ++tb) *(LAS v2u*)(vrow + tb * 2560) = ones ? v2u{0x00003F80u, 0u} : v2u{bfr[tb].z, bfr[tb].w};
        }
        SB();
        constexpr float L2E = 1.4426950408889634f;
        f32x2 p01[4], p23[4]; float p4[4], mx[4], mn[4];
#pragma unroll
        for (int tb = 0; tb < 4; ++tb) {
          p01[tb] = f32x2{pa[tb].x, pa[tb].y} * L2E; p23[tb] = f32x2{pa[tb].z, pa[tb].w} * L2E; p4[tb] = pb[tb].x * L2E;
          mx[tb] = fmax3_raw(fmax3_raw(p01[tb].x, p01[tb].y, p23[tb].x), p23[tb].y, p4[tb]);
          mn[tb] = -fmin3_raw(fmin3_raw(p01[tb].x, p01[tb].y, p23[tb].x), p23[tb].y, p4[tb]);
        }
        xr4<true>(mx, mn);
        SB();
        WO_GROUP(1)
        float e[4][5], f[4][5], sp[4], sm[4];
#pragma unroll
        for (int tb = 0; tb < 4; ++tb) {
          const f32x2 a = p01[tb] - mx[tb], b = p23[tb] - mx[tb], cc = p01[tb] + mn[tb], d = p23[tb] + mn[tb];
          e[tb][0] = __builtin_amdgcn_exp2f(a.x); e[tb][1] = __builtin_amdgcn_exp2f(a.y); e[tb][2] = __builtin_amdgcn_exp2f(b.x);
          e[tb][3] = __builtin_amdgcn_exp2f(b.y); e[tb][4] = __builtin_amdgcn_exp2f(p4[tb] - mx[tb]);
          f[tb][0] = __builtin_amdgcn_exp2f(-cc.x); f[tb][1] = __builtin_amdgcn_exp2f(-cc.y); f[tb][2] = __builtin_amdgcn_exp2f(-d.x);
          f[tb][3] = __builtin_amdgcn_exp2f(-d.y); f[tb][4] = __builtin_amdgcn_exp2f(-(p4[tb] + mn[tb]));
        }
#pragma unroll
        for (int tb = 0; tb < 4; ++tb) {
          const f32x2 s2 = (f32x2{e[tb][0], e[tb][1]} + f32x2{e[tb][2], e[tb][3]}), s3 = (f32x2{f[tb][0], f[tb][1]} + f32x2{f[tb][2], f[tb][3]});
          sp[tb] = (s2.x + s2.y) + e[tb][4]; sm[tb] = (s3.x + s3.y) + f[tb][4];
        }
        xr4<false>(sp, sm);
        SB();
        WO_GROUP(2)
#pragma unroll
        for (int tb = 0; tb < 4; ++tb) {
          const float rp = __builtin_amdgcn_rcpf(sp[tb]), rm = __builtin_amdgcn_rcpf(sm[tb]);
          phq[tb][0] = pk2v(f32x2{e[tb][0], e[tb][1]} * rp); phq[tb][1] = pk2v(f32x2{e[tb][2], e[tb][3]} * rp);
          phq[tb][2] = pk2(e[tb][4] * rp, f[tb][0] * rm);
          phq[tb][3] = pk2v(f32x2{f[tb][1], f[tb][2]} * rm); phq[tb][4] = pk2v(f32x2{f[tb][3], f[tb][4]} * rm);
        }
        WO_GROUP(3)
#undef WO_GROUP
        if (role == 1) {
          LAS unsigned char* row = img + t * 160 + 24 * g;
#pragma unroll
          for (int tb = 0; tb < 4; ++tb) {
            *(LAS v2u*)(row + tb * 2560) = v2u{phq[tb][0], phq[tb][1]};
            *(LAS v2u*)(row + tb * 2560 + 8) = v2u{phq[tb][2], phq[tb][3]};
            *(LAS v2u*)(row + tb * 2560 + 16) = v2u{phq[tb][4], 0u};
          }
        }
      }
      FST(25)
      BAR_L();
      {
        const unsigned lb = tr_row(lane) * 160 + (lane & 3) * 8;
        f32x4 kv[3];
#pragma unroll
        for (int cb = 0; cb < 3; ++cb) kv[cb] = f32x4{0.f, 0.f, 0.f, 0.f};
#pragma unroll
        for (int ks = 0; ks < 2; ++ks) {
          const v4u bv = trfrag<1280>(img + lb + ks * 5120 + 96 + 32 * role);
#pragma unroll
          for (int cb = 0; cb < 3; ++cb) kv[cb] = mfma(trfrag<1280>(img + lb + ks * 5120 + 32 * cb), bv, kv[cb]);
        }
        const unsigned so = WS_PART + ly * PART_L + (unsigned)(c * 32 + r) * 16384u + j * 4096u;
        const unsigned vo2 = role == 0 ? v16 : (t < 5 ? (unsigned)(3072 + (g * 5 + t) * 16) : OOB);
        const unsigned st = role == 0 ? 1024u : 320u;
        const u64 tg = tag_of(k, ph0 + PH_KV1);
#pragma unroll
        for (int cb = 0; cb < 3; ++cb) st128(granule(kv[cb], tg), vo2, ws, so + cb * st);
      }
      FST(26)
      stamp(k, sb + 2);
      if (w == 7) {
        const unsigned so = WS_PART + ly * PART_L + (unsigned)(c * 32) * 16384u + r * 512u;
        const unsigned vo2 = (unsigned)((lane >> 5) * 16384 + (lane & 31) * 16);
        const bool hole = (r & 7) == 7 && (lane & 31) >= 28;
        const u64 tg = tag_of(k, ph0 + PH_KV1);
        v4u pr[16];
        if (FINE == 4 && ly == 1) stampw(k, 128);
        unsigned spins = 0;
        for (;;) {
          bool ok = true;
#pragma unroll
          for (int i = 0; i < 16; ++i) pr[i] = __builtin_amdgcn_raw_buffer_load_b128(ws, vo2, so + i * 32768u, 16);
#pragma unroll
          for (int i = 0; i < 16; ++i) ok = ok && granule_ok(pr[i], tg);
          if (__all(ok || hole) || !spin_more(k, spins)) break;
        }
        if (FINE == 4 && ly == 1) { stampw(k, 136); if (lane == 0) *(LAS u64*)(k.L + L_MISC + 2048 + 152 * 8) = spins; }
        f32x4 sum = {0.f, 0.f, 0.f, 0.f};
#pragma unroll
        for (int i = 0; i < 16; ++i) sum += f32x4{bflo(pr[i].x), bfhi(pr[i].x), bflo(pr[i].z), bfhi(pr[i].z)};
#pragma unroll
        for (int e = 0; e < 4; ++e) {
          auto sw = __builtin_amdgcn_permlane32_swap(asu(sum[e]), asu(sum[e]), false, false);
          sum[e] = asf(sw[0]) + asf(sw[1]);
        }
        st128(granule(sum, tag_of(k, ph0 + PH_KV2)), lane < 32 ? v16 : OOB, ws, WS_RED + (unsigned)(ly * 8 + c) * 16384u + r * 512u);
        stamp(k, sb + 3);
      } else if (role == 0) {
        const unsigned so = WS_RED + (unsigned)(ly * 8 + c) * 16384u + j * 4096u;
        const int dd = t < 4 ? t : ((t & 3) == 0 ? 4 : -1);
        const int Q0 = 20 * c + 5 * j + g, Q1 = 20 * c + 5 * j + 4;
        const unsigned o0 = pinv(frag_off(Q0 >> 2, Q0 & 3, t)), o1 = pinv(g == 0 ? frag_off(Q1 >> 2, Q1 & 3, t) : OOB);
        const u64 tg = tag_of(k, ph0 + PH_KV2);
        v4u l0[3], l1[3];
        if (FINE == 4 && ly == 1) stampw(k, 128);
        unsigned spins = 0;
        for (;;) {
          bool ok = true;
#pragma unroll
          for (int u = 0; u < 3; ++u) {
            const int qc = 3 * g + u;
            l0[u] = __builtin_amdgcn_raw_buffer_load_b128(ws, (unsigned)((qc >> 2) * 1024 + ((qc & 3) * 16 + t) * 16), so, 16);
            l1[u] = __builtin_amdgcn_raw_buffer_load_b128(ws, dd >= 0 ? (unsigned)(3072 + (qc * 5 + dd) * 16) : OOB, so, 16);
          }
#pragma unroll
          for (int u = 0; u < 3; ++u) ok = ok && granule_ok(l0[u], tg) && (dd < 0 || granule_ok(l1[u], tg));
          if (__all(ok) || !spin_more(k, spins)) break;
        }
        if (FINE == 4 && ly == 1) { stampw(k, 136); if (lane == 0 && w == 0) *(LAS u64*)(k.L + L_MISC + 2048 + 153 * 8) = spins; }
        stamp(k, sb + 3);
        const v4u a00 = {l0[0].x, l0[0].z, l0[1].x, l0[1].z}, a01 = {l0[2].x, l0[2].z, 0u, 0u};
        const v4u a10 = {l1[0].x, l1[0].z, l1[1].x, l1[1].z}, a11 = {l1[2].x, l1[2].z, 0u, 0u};
        f32x4 n0[4], n1[4];
        const f32x4 z = {0.f, 0.f, 0.f, 0.f};
#pragma unroll
        for (int tb = 0; tb < 4; ++tb) {
          const v4u b0 = {phq[tb][0], phq[tb][1], phq[tb][2], phq[tb][3]}, b1 = {phq[tb][4], 0u, 0u, 0u};
          n0[tb] = mfma(a01, b1, mfma(a00, b0, z)); n1[tb] = mfma(a11, b1, mfma(a10, b0, z));
        }
#pragma unroll
        for (int tb = 0; tb < 4; ++tb) {
          auto sw = __builtin_amdgcn_permlane16_swap(asu(n1[tb].x), asu(n1[tb].x), false, false);
          const float rd = __builtin_amdgcn_rcpf((g == 0 ? asf(sw[1]) : n1[tb].x) + 1e-6f);
          const unsigned so2 = act + A_O + (unsigned)r * 81920u + tb * 1024u;
          st64(pk4(n0[tb] * rd), o0, ws, so2);
          st64(pk4(n1[tb] * rd), o1, ws, so2);
        }
        WAIT_VM(0);
      }
      BAR();
      post_flag(k, fl_group(ph0 + PH_O, r, c), tag_of(k, ph0 + PH_O));
      stamp(k, sb + 4);
    }
    if (STOP_PHASE == layer * 7 + 0) return;
    {
      PHASE_VARS()
      const int lane = opq(lane0);
      const unsigned v16 = pinv(lane * 16), xo = kx_off(k, act + A_O);
      const RingAddr ra = ring_addr(k, lane);
      const unsigned pa1 = (unsigned)(2 * w < 13 ? 2 * w : 13) * 1024u, pl1 = pin(140u * 1024u + pa1), ps1 = pin(wt + WT_1 + pa1);
      const unsigned pl2 = pin(5u * w * 1024u), ps2 = pin(wt + WT_1 + (15u + 5u * w) * 1024u);
      const unsigned pa3 = (unsigned)(6 * w < 39 ? 6 * w : 39) * 1024u, pl3 = pin(40u * 1024u + pa3), ps3 = pin(wt + WT_1 + 55u * 1024u + pa3);
      poll_flags(k, fl_group(ph0 + PH_O, r, lane & 7), tag_of(k, ph0 + PH_O));
      BAR_VL0();
      stamp(k, sb + 5);
      v4u xf[10];
      kx_issue(xf, ws, xo, v16);
      f32x4 acc[5];
#pragma unroll
      for (int bl = 0; bl < 5; ++bl) acc[bl] = f32x4{0.f, 0.f, 0.f, 0.f};
#define C_PF(p) { if ((p) < 2) gl1<(p) & 1>(pl1, v16, ws, ps1); else gl1<((p) >= 2 ? (p) - 2 : 0)>(pl2, v16, ws, ps2); }
      KMAIN(P_C, 0, 7, C_PF)
#undef C_PF
      BAR_L();
      stamp(k, sb + 6);
      gln<2>(pl3, v16, ws, ps3);
      f32x4 fin[3];
      kexchange(k, acc, fin, 17 * SLOT, lane);
      gln<2>(pl3 + 2048, v16, ws, ps3 + 2048);
      v2u pk[3];
#pragma unroll
      for (int j = 0; j < 3; ++j) { pk[j] = pk4(xres[j] + rnd4(fin[j])); x2r[j] = unpk(pk[j]); }
      gln<2>(pl3 + 4096, v16, ws, ps3 + 4096);
      kstore(k, ws, act + A_X2, pk, lane);
      kstats(k, ws, ly * 3 + 0, x2r, lane);
      FST(28)
      BAR_VL0();
      post_flag(k, fl_group(ph0 + PH_X2, r, c), tag_of(k, ph0 + PH_X2));
      stamp(k, sb + 7);
    }
    if (STOP_PHASE == layer * 7 + 1) return;
    {
      PHASE_VARS()
      const int lane = opq(lane0), t = lane & 15, g = lane >> 4, kh = w >> 2;
      const unsigned v16 = pinv(lane * 16), xo = kx_off(k, act + A_X2);
      const unsigned co = pinv((unsigned)(1920 + 80 * c + 48 * kh + 4 * g) * 4u);
      const RingAddr ra = ring_addr(k, lane);
      const int shift = (c >> 1) == 0 ? 1 : (c >> 1) == 1 ? -1 : (c >> 1) == 2 ? 2 : -2;
      const int tok = r * 64 + (w & 3) * 16 + t, dst = tok + shift;
      const unsigned dof = pinv((dst >= 0 && dst < S_) ? (unsigned)(dst >> 6) * 81920u + (unsigned)((dst >> 4) & 3) * 1024u + frag_off(0, g, dst & 15) : OOB);
      const unsigned pa1 = (unsigned)(7 * w < 48 ? 7 * w : 48) * 1024u, pl1 = pin(85u * 1024u + pa1), ps1 = pin(wt + WT_2 + pa1);
      const unsigned pa2 = (unsigned)(2 * w < 13 ? 2 * w : 13) * 1024u, pl2 = pin(140u * 1024u + pa2), ps2 = pin(wt + WT_2 + 55u * 1024u + pa2);
      const unsigned pa3 = (unsigned)(4 * w < 26 ? 4 * w : 26) * 1024u, pl3 = pin(pa3), ps3 = pin(wt + WT_2 + 70u * 1024u + pa3);
      poll_flags(k, fl_group(ph0 + PH_X2, r, lane & 7), tag_of(k, ph0 + PH_X2));
      BAR();
      stamp(k, sb + 8);
      v4u xf[10];
      kx_issue(xf, ws, xo, v16);
      StatLd st; stat_issue(st, ws, ly * 3 + 0, r * 4 + (w & 3), lane);
      v4u cs[3];
      ld128<0>(cs[0], co, ws, WS_CS); ld128<64>(cs[1], co, ws, WS_CS); ld128<128>(cs[2], co, ws, WS_CS);
      f32x4 acc[5];
#pragma unroll
      for (int bl = 0; bl < 5; ++bl) acc[bl] = f32x4{0.f, 0.f, 0.f, 0.f};
#define D_PF(p) gl1<p>(pl1, v16, ws, ps1);
      KMAIN(P_D, 7, 7, D_PF)
#undef D_PF
      asm volatile("s_waitcnt vmcnt(7)" : "+v"(st.s0), "+v"(st.s1), "+v"(st.s2), "+v"(st.s3), "+v"(cs[0]), "+v"(cs[1]), "+v"(cs[2]));
      float mean, var; stat_finish(st, mean, var);
      const float rstd = __builtin_amdgcn_rsqf(var + 1e-5f);
      const float sc2 = rstd * __builtin_amdgcn_rsqf(var * rstd * rstd + 1e-5f);
      BAR_L();
      stamp(k, sb + 9);
      gln<2>(pl2, v16, ws, ps2);
      f32x4 fin[3];
      kexchange(k, acc, fin, 6 * SLOT, lane);
      gln<2>(pl3, v16, ws, ps3);
#pragma unroll
      for (int j = 0; j < 3; ++j) {
        const int B = 5 * c + 3 * kh + j;
        if (!(j == 2 && kh)) st64(ln_gelu4(fin[j], __builtin_bit_cast(f32x4, cs[j]), -mean, sc2), dof, ws, act + A_HS + blk_off(B));
      }
      gln<2>(pl3 + 2048, v16, ws, ps3 + 2048);
      if (r == 0 || r == 31) {
        const bool z = shift > 0 ? tok < shift : tok >= S_ + shift;
        const unsigned zof = z ? (unsigned)(tok >> 6) * 81920u + (unsigned)((tok >> 4) & 3) * 1024u + frag_off(0, g, tok & 15) : OOB;
#pragma unroll
        for (int j = 0; j < 3; ++j) {
          const int B = 5 * c + 3 * kh + j;
          if (!(j == 2 && kh)) st64(v2u{0u, 0u}, zof, ws, act + A_HS + blk_off(B));
        }
      }
#pragma unroll
      for (int j = 0; j < 3; ++j) x1r[j] = rnd4((x2r[j] - mean) * rstd);
      BAR_VL0();
      post_flag(k, fl_group(ph0 + PH_H, r, c), tag_of(k, ph0 + PH_H));
      stamp(k, sb + 10);
    }
    if (STOP_PHASE == layer * 7 + 2) return;
#define F_WLOAD(i) { ld128<0>(wf3[(i) % 4][0], v16, ws, wa3 + (i) * 12288u); ld128<1024>(wf3[(i) % 4][1], v16, ws, wa3 + (i) * 12288u); \
                     ld128<2048>(wf3[(i) % 4][2], v16, ws, wa3 + (i) * 12288u); }
    {
      PHASE_VARS()
      const int lane = opq(lane0);
      const unsigned v16 = pinv(lane * 16), xo = kx_off(k, act + A_HS);
      const RingAddr ra = ring_addr(k, lane);
      const unsigned pa1 = (unsigned)(6 * w < 39 ? 6 * w : 39) * 1024u, pl1 = pin(P_G * 5u * 1024u + pa1), ps1 = pin(wt + WT_4 + pa1);
      { const int rr = r + ((lane >> 3) % 3) - 1;
        poll_flags(k, fl_group(ph0 + PH_H, rr < 0 ? 0 : (rr > 31 ? 31 : rr), lane & 7), tag_of(k, ph0 + PH_H)); }
      BAR();
      stamp(k, sb + 11);
      v4u xf[10];
      kx_issue(xf, ws, xo, v16);
      f32x4 acc[5];
#pragma unroll
      for (int bl = 0; bl < 5; ++bl) acc[bl] = f32x4{0.f, 0.f, 0.f, 0.f};
#define E_PF(p) gl1<p>(pl1, v16, ws, ps1);
      KMAIN(P_E, 0, 6, E_PF)
#undef E_PF
      BAR_L();
      stamp(k, sb + 12);
      f32x4 fin[3];
      kexchange(k, acc, fin, 17 * SLOT, lane);
      v2u pk[3];
#pragma unroll
      for (int j = 0; j < 3; ++j) { pk[j] = pk4(x1r[j] + rnd4(fin[j])); x1r[j] = unpk(pk[j]); }
      kstore(k, ws, act + A_X1, pk, lane);
      kstats(k, ws, ly * 3 + 1, x1r, lane);
      BAR_VL0();
      post_flag(k, fl_group(ph0 + PH_X1, r, c), tag_of(k, ph0 + PH_X1));
      stamp(k, sb + 13);
    }
    if (STOP_PHASE == layer * 7 + 3) return;
    {
      PHASE_VARS()
      const int lane = opq(lane0), t = lane & 15, g = lane >> 4, cp = w & 3, kh = w >> 2;
      const unsigned v16 = pinv(lane * 16);
      const unsigned wa3 = pin(wt + WT_3 + (unsigned)(kh * 10) * 12288u + cp * 3072u);
      const unsigned co = pinv((unsigned)(2560 + 192 * c + 48 * cp + 4 * g) * 4u);
      const int ka = (w & 1) + ((w >> 1) & 1) * 10 + (w >> 2) * 5;
      const unsigned xa = pin((unsigned)ka * 4096u), xb2 = pin((unsigned)(4 + ((w >> 1) & 1) * 10 + (w >> 2) * 5) * 4096u + (w & 1) * 2048u);
      const unsigned xsrc = pin(act + A_X1 + (unsigned)r * 81920u);
      v4u wf3[4][3];
#pragma unroll
      for (int i = 0; i < 4; ++i) asm volatile("" : "=v"(wf3[i][0]), "=v"(wf3[i][1]), "=v"(wf3[i][2]));
#define F_WINDOW() { F_WLOAD(0) }
      if (w != 7) F_WINDOW()
      poll_flags(k, fl_group(ph0 + PH_X1, r, lane & 7), tag_of(k, ph0 + PH_X1));
      BAR();
      FST(41)
      gln<4>(L_XF + xa, v16, ws, xsrc + xa); gln<4>(L_XF + xa + 8192u, v16, ws, xsrc + xa + 8192u); gln<2>(L_XF + xb2, v16, ws, xsrc + xb2);
      if (w == 7) F_WINDOW()
#undef F_WINDOW
      f32x4 acc[3][4];
#pragma unroll
      for (int bl = 0; bl < 3; ++bl)
#pragma unroll
        for (int tb = 0; tb < 4; ++tb) acc[bl][tb] = f32x4{0.f, 0.f, 0.f, 0.f};
      if (w < 4) WAIT_VM(6); else if (w < 7) WAIT_VM(10);
      BAR();
      StatLd st;
      v4u cs[3];
      F_WLOAD(1) F_WLOAD(2) F_WLOAD(3)
      stamp(k, sb + 14);
      FST(29)
      v4u xf[2][4];
      LAS unsigned char* xb = k.L + L_XF + v16 + kh * 40960;
#define F_READ(i) { _Pragma("unroll") for (int tb = 0; tb < 4; ++tb) xf[(i) & 1][tb] = *(LAS v4u*)(xb + ((i) * 4 + tb) * 1024); }
#define F_IT(i, N)                                                                                    \
  {                                                                                                   \
    wait_tie3<N>(wf3[(i) % 4][0], wf3[(i) % 4][1], wf3[(i) % 4][2]);                                  \
    if ((i) == 1) BAR();                    \
    if ((i) + 1 < 10) F_READ((i) + 1)                                                                 \
    _Pragma("unroll") for (int tb = 0; tb < 4; ++tb)                                                  \
      _Pragma("unroll") for (int bl = 0; bl < 3; ++bl) acc[bl][tb] = mfma(wf3[(i) % 4][bl], xf[(i) & 1][tb], acc[bl][tb]); \
    if ((i) + 1 < 10) MIX(3, 4)                                                                       \
    SB();                                                                                             \
    if ((i) + 4 < 10) F_WLOAD((i) + 4)                                                                \
    if ((i) == 1) { stat_issue(st, ws, ly * 3 + 1, r * 4 + (w & 3), lane);                            \
                    ld128<0>(cs[0], co, ws, WS_CS); ld128<64>(cs[1], co, ws, WS_CS); ld128<128>(cs[2], co, ws, WS_CS); } \
  }
      F_READ(0)
#define F_W7() if (w == 7) WAIT_VM(9);
      F_W7() F_IT(0, 63) F_IT(1, 9) FST(30) F_IT(2, 16) FST(31) F_IT(3, 16) F_IT(4, 16) FST(32) F_IT(5, 16) F_IT(6, 9) F_IT(7, 6) F_IT(8, 3) F_IT(9, 0)
#undef F_IT
#undef F_W7
#undef F_READ
      {
        wait_tie_stat<0>(st);
        float mean, var; stat_finish(st, mean, var);
        if (lane < 16) *(LAS f32x2*)(k.L + L_STATS + ((w & 3) * 16 + lane) * 8) = f32x2{mean, __builtin_amdgcn_rsqf(var + 1e-5f)};
      }
      BAR_L();
      stamp(k, sb + 15);
      f32x4 fin[3][2];
      {
        LAS f32x4* sc = (LAS f32x4*)(k.L + L_XF + v16);
        wstream_exact<4>(k, ws, wt + WT_4 + 125 * 1024, 0, 30, v16);
        if (kh == 0) {
#pragma unroll
          for (int bl = 0; bl < 3; ++bl) { sc[((cp * 2 + 1) * 6 + bl * 2) * 64] = acc[bl][2]; sc[((cp * 2 + 1) * 6 + bl * 2 + 1) * 64] = acc[bl][3];
                                           fin[bl][0] = acc[bl][0]; fin[bl][1] = acc[bl][1]; }
        } else {
#pragma unroll
          for (int bl = 0; bl < 3; ++bl) { sc[((cp * 2) * 6 + bl * 2) * 64] = acc[bl][0]; sc[((cp * 2) * 6 + bl * 2 + 1) * 64] = acc[bl][1];
                                           fin[bl][0] = acc[bl][2]; fin[bl][1] = acc[bl][3]; }
        }
        BAR_L();
#pragma unroll
        for (int bl = 0; bl < 3; ++bl) { fin[bl][0] += sc[((cp * 2 + kh) * 6 + bl * 2) * 64]; fin[bl][1] += sc[((cp * 2 + kh) * 6 + bl * 2 + 1) * 64]; }
        BAR_L();
      }
      FST(33)
      const unsigned w4l = pin((75u + 10u * w) * 1024u), w4g = pin(wt + WT_4 + (45u + 10u * w) * 1024u);
      tie3(cs[0], cs[1], cs[2]);
      const unsigned lo = frag_off(0, g, t);
#pragma unroll
      for (int u = 0; u < 2; ++u) {
        const int tb = 2 * kh + u;
        const f32x2 ms = *(LAS f32x2*)(k.L + L_STATS + (tb * 16 + t) * 8);
#pragma unroll
        for (int bl = 0; bl < 3; ++bl) {
          const int B = 12 * c + 3 * cp + bl;
          if (u == 0 && bl == 0) gln<4>(w4l, v16, ws, w4g);
          if (u == 0 && bl == 2) gln<4>(w4l + 4096, v16, ws, w4g + 4096);
          if (u == 1 && bl == 1) gln<2>(w4l + 8192, v16, ws, w4g + 8192);
          st64(ln_gelu4(fin[bl][u], __builtin_bit_cast(f32x4, cs[bl]), -ms.x, ms.y), lo, ws,
               act + A_H2 + (unsigned)r * 196608u + tb * 1024u + blk_off(B));
        }
      }
      FST(34)
      BAR_VL0();
      post_flag(k, fl_group(ph0 + PH_H2, r, c), tag_of(k, ph0 + PH_H2));
      stamp(k, sb + 16);
    }
#undef F_WLOAD
    if (STOP_PHASE == layer * 7 + 4) return;
    {
      PHASE_VARS()
      const int lane = opq(lane0), t = lane & 15, g = lane >> 4, kh = w >> 2;
      const unsigned v16 = pinv(lane * 16);
      const unsigned xo = pin(act + A_H2 + (unsigned)r * 196608u + (unsigned)kh * 98304u + (unsigned)(w & 3) * 1024u);
      const RingAddr ra = ring_addr(k, lane);
      const unsigned w4s = pin(wt + WT_4 + (unsigned)w * SLOT);
      poll_flags(k, fl_group(ph0 + PH_H2, r, lane & 7), tag_of(k, ph0 + PH_H2));
      BAR();
      stamp(k, sb + 17);
      v4u xf[24];
#define GX(i) ld128<0>(xf[i], v16, ws, xo + (i) * 4096u);
      GX(0) GX(1) GX(2) GX(3) GX(4) GX(5) GX(6) GX(7)
      f32x4 acc[5];
#pragma unroll
      for (int bl = 0; bl < 5; ++bl) acc[bl] = f32x4{0.f, 0.f, 0.f, 0.f};
      v4u wf[2][5];
#define G_MMA(i) { _Pragma("unroll") for (int bl = 0; bl < 5; ++bl) acc[bl] = mfma(wf[(i) & 1][bl], xf[i], acc[bl]); }
#define G_STEP(i)                                                                                     \
  {                                                                                                   \
    if ((i) >= 1) wait_tie<g_xwait((i) >= 1 ? (i) : 1)>(xf[(i) >= 1 ? (i) - 1 : 0]);                  \
    ring_read<P_G + 2 * (i)>(ra, wf[(i) & 1]);                                                        \
    if ((i) >= 1) { G_MMA((i) >= 1 ? (i) - 1 : 0) MIX(1, 5) }                                         \
    SB();                                                                                             \
    if ((i) + 8 < 24) GX((i) + 8 < 24 ? (i) + 8 : 0)                                                  \
  }
      G_STEP(0) G_STEP(1) FST(35) G_STEP(2) G_STEP(3)
      FST(36)
      BAR_L();
      {
        const unsigned l = (unsigned)(6 + w) * SLOT, g = w4s + 31 * SLOT;
        G_STEP(4) gl1<0>(l, v16, ws, g); gl1<1>(l, v16, ws, g); G_STEP(5) gl1<2>(l, v16, ws, g); G_STEP(6) gl1<3>(l, v16, ws, g); G_STEP(7) gl1<4>(l, v16, ws, g);
      }
      BAR_L();
      {
        const unsigned l = (unsigned)(14 + w) * SLOT, g = w4s + 39 * SLOT;
        G_STEP(8) gl1<0>(l, v16, ws, g); gl1<1>(l, v16, ws, g); G_STEP(9) gl1<2>(l, v16, ws, g); G_STEP(10) gl1<3>(l, v16, ws, g); G_STEP(11) gl1<4>(l, v16, ws, g);
      }
      FST(37)
      bar_vl<9>();
      if (w == 0) gln<5>(22 * SLOT, v16, ws, w4s + 47 * SLOT);
      G_STEP(12) G_STEP(13) G_STEP(14) G_STEP(15)
      bar_vl<4>();
      G_STEP(16) G_STEP(17) G_STEP(18) G_STEP(19)
      FST(38)
      bar_vl<0>();
      G_STEP(20) G_STEP(21) G_STEP(22) G_STEP(23)
      tie1(xf[23]);
      G_MMA(23)
#undef G_STEP
#undef G_MMA
#undef GX
      BAR_L();
      stamp(k, sb + 18);
      f32x4 fin[3];
      kexchange(k, acc, fin, 24 * SLOT, lane);
      v2u pk[3];
#pragma unroll
      for (int j = 0; j < 3; ++j) { pk[j] = pk4(x2r[j] + rnd4(x1r[j] + rnd4(fin[j]))); xres[j] = unpk(pk[j]); }
      if (ly == 0) {
        kstore(k, ws, WS_ACT + A_XO, pk, lane);
        kstats(k, ws, 2, xres, lane);
        BAR_VL0();
        post_flag(k, fl_group(PH_XO, r, c), tag_of(k, PH_XO));
      } else {
        unsigned char* po = opqp(out) + (size_t)(r * 64 + (w & 3) * 16 + t) * 1280 + (80 * c + 48 * kh + 4 * g) * 2;
#pragma unroll
        for (int j = 0; j < 3; ++j)
          if (!(j == 2 && kh)) *(GAS v2u*)(po + 32 * j) = pk[j];
      }
      stamp(k, sb + 19);
    }
    if (STOP_PHASE == layer * 7 + 5) return;
  }
  stampc(k, 127); stamp(k, 125); stamp_dump(k);
  if (k.b == 0 && tid == 0) __hip_atomic_store((GAS u64*)(wsp + WS_EPOCH), k.tag + 64, __ATOMIC_RELAXED, __HIP_MEMORY_SCOPE_AGENT);
}

extern "C" void kernel_launch(void* const* d_in, const int* in_sizes, int n_in, void* d_out, int out_size, void* d_ws, size_t ws_size,
                              hipStream_t stream) {
  (void)in_sizes; (void)n_in; (void)out_size; (void)ws_size;
  (void)hipFuncSetAttribute((const void*)hedgehog_smlp_kernel, hipFuncAttributeMaxDynamicSharedMemorySize, LDS_BYTES);
  hedgehog_smlp_kernel<<<256, 512, LDS_BYTES, stream>>>(
      (const unsigned char*)d_in[0], (const unsigned char*)d_in[1], (const unsigned char*)d_in[2], (const unsigned char*)d_in[3],
      (const unsigned char*)d_in[5], (const unsigned char*)d_in[6], (const unsigned char*)d_in[7], (const unsigned char*)d_in[8],
      (unsigned char*)d_out, (unsigned char*)d_ws);
}
```

```cpp
#include <hip/hip_runtime.h>
#include <hip/hip_bf16.h>
#include <stdint.h>

#define DI __device__ __forceinline__
#define LAS __attribute__((address_space(3)))
#define GAS __attribute__((address_space(1)))
typedef unsigned v4u __attribute__((ext_vector_type(4)));
typedef unsigned v2u __attribute__((ext_vector_type(2)));
typedef float f32x4 __attribute__((ext_vector_type(4)));
typedef float f32x2 __attribute__((ext_vector_type(2)));
typedef short bf16x8 __attribute__((ext_vector_type(8)));
typedef short s16x4 __attribute__((ext_vector_type(4)));
typedef __bf16 bf2_t __attribute__((ext_vector_type(2)));
typedef __amdgpu_buffer_rsrc_t srd_t;
typedef unsigned long long u64;

#define STOP_PHASE 1000
#define STAMPS 0
#define FINE 0
#define LATEW(w) ((w) == 7)
#define XDELAY 14
#define GDELAY 28
#define X0N 8
#define FST(id) { if (FINE) stamp(k, sb + (id)); }

constexpr int S_ = 2048, D_ = 640, F_ = 1536;
constexpr unsigned WS_EPOCH = 0, WS_TMO = 64;
constexpr unsigned WS_FLAGS = 4096;
constexpr unsigned WS_PCS = 512u << 10;
constexpr unsigned PCS_C = 20 * 33 * 64;
constexpr unsigned WS_STAMP = 131072;
constexpr unsigned WS_RED = 1u << 20;
constexpr unsigned WS_STAT = WS_RED + (1u << 19);
constexpr unsigned STAT_SZ = 128 * 1024;
constexpr unsigned WS_PART = 4u << 20;
constexpr unsigned PART_L = 256u * 16384u;
constexpr unsigned ACT20 = 128u * 20u * 1024u, ACT48 = 128u * 48u * 1024u;
constexpr unsigned WS_ACT = 16u << 20;
constexpr unsigned ACT_L = 5u * ACT20 + ACT48;
constexpr unsigned A_O = 0, A_X2 = ACT20, A_HS = 2 * ACT20, A_X1 = 3 * ACT20, A_XO = 4 * ACT20, A_H2 = 5 * ACT20;
constexpr unsigned WS_WT = 64u << 20;
constexpr unsigned WT_A = 0, WT_O = 327680, WT_1 = WT_O + 102400, WT_2 = WT_1 + 102400, WT_4 = WT_2 + 102400, WT_3 = WT_4 + 245760,
                   WT_C = WT_3 + 245760;
constexpr unsigned WS_END = WS_WT + 8 * WT_C;
enum { PH_KV1 = 0, PH_KV2, PH_O, PH_X2, PH_H, PH_X1, PH_H2, PH_XO, PH_PER_LAYER, PH_T = 16, PH_TG = 17 };
constexpr unsigned SLOT = 5120, NSLOT = 31, RINGP = 155;
constexpr unsigned L_MISC = SLOT * NSLOT;
constexpr unsigned L_STATS = L_MISC;
constexpr unsigned L_CSL = L_MISC + 512;
constexpr unsigned LDS_BYTES = 163840;
constexpr unsigned L_X = 0, L_XF = 76800;
constexpr unsigned L_IMG = 0, IMG_H = 10240;
constexpr unsigned L_S0 = 81920, L_SJ = 97280, L_SPH = 146432;
constexpr int P_C = 8, P_D = 28, P_E = 17, P_G = 6;
constexpr unsigned OOB = 0x80000000u;

#define WAIT_VM(n) asm volatile("s_waitcnt vmcnt(" #n ")" ::: "memory")
#define BAR() asm volatile("s_barrier" ::: "memory")
#define BAR_L() asm volatile("s_waitcnt lgkmcnt(0)\n\ts_barrier" ::: "memory")
#define BAR_VL0() asm volatile("s_waitcnt vmcnt(0) lgkmcnt(0)\n\ts_barrier" ::: "memory")
#define SB() __builtin_amdgcn_sched_barrier(0)
template <int N> DI void bar_vl() { asm volatile("s_waitcnt vmcnt(%0) lgkmcnt(0)\n\ts_barrier" ::"i"(N) : "memory"); }

DI void glds(unsigned lds, unsigned voff, srd_t srd, unsigned soff) {
  asm volatile("s_mov_b32 m0, %0\n\ts_nop 0\n\tbuffer_load_dwordx4 %1, %2, %3 offen lds" ::"s"(lds), "v"(voff), "s"(srd), "s"(soff) : "memory");
}
template <int N> DI void gln(unsigned lds, unsigned voff, srd_t srd, unsigned soff);
template <> DI void gln<1>(unsigned lds, unsigned voff, srd_t srd, unsigned soff) {
  asm volatile("s_mov_b32 m0, %0\n\ts_nop 0\n\tbuffer_load_dwordx4 %1, %2, %3 offen sc1 lds" ::"s"(lds), "v"(voff), "s"(srd), "s"(soff) : "memory");
}
template <> DI void gln<2>(unsigned lds, unsigned voff, srd_t srd, unsigned soff) {
  asm volatile("s_mov_b32 m0, %0\n\ts_nop 0\n\tbuffer_load_dwordx4 %1, %2, %3 offen sc1 lds\n\t"
               "buffer_load_dwordx4 %1, %2, %3 offen offset:1024 sc1 lds" ::"s"(lds), "v"(voff), "s"(srd), "s"(soff) : "memory");
}
template <> DI void gln<3>(unsigned lds, unsigned voff, srd_t srd, unsigned soff) {
  asm volatile("s_mov_b32 m0, %0\n\ts_nop 0\n\tbuffer_load_dwordx4 %1, %2, %3 offen sc1 lds\n\t"
               "buffer_load_dwordx4 %1, %2, %3 offen offset:1024 sc1 lds\n\tbuffer_load_dwordx4 %1, %2, %3 offen offset:2048 sc1 lds"
               ::"s"(lds), "v"(voff), "s"(srd), "s"(soff) : "memory");
}
template <> DI void gln<4>(unsigned lds, unsigned voff, srd_t srd, unsigned soff) {
  asm volatile("s_mov_b32 m0, %0\n\ts_nop 0\n\tbuffer_load_dwordx4 %1, %2, %3 offen sc1 lds\n\t"
               "buffer_load_dwordx4 %1, %2, %3 offen offset:1024 sc1 lds\n\tbuffer_load_dwordx4 %1, %2, %3 offen offset:2048 sc1 lds\n\t"
               "buffer_load_dwordx4 %1, %2, %3 offen offset:3072 sc1 lds" ::"s"(lds), "v"(voff), "s"(srd), "s"(soff) : "memory");
}
template <> DI void gln<6>(unsigned lds, unsigned voff, srd_t srd, unsigned soff);
template <> DI void gln<5>(unsigned lds, unsigned voff, srd_t srd, unsigned soff) { gln<4>(lds, voff, srd, soff); gln<1>(lds + 4096, voff, srd, soff + 4096); }
template <> DI void gln<6>(unsigned lds, unsigned voff, srd_t srd, unsigned soff) { gln<4>(lds, voff, srd, soff); gln<2>(lds + 4096, voff, srd, soff + 4096); }
template <int P> DI void gl1(unsigned lds, unsigned voff, srd_t srd, unsigned soff) {
  asm volatile("s_mov_b32 m0, %0\n\ts_nop 0\n\tbuffer_load_dwordx4 %1, %2, %3 offen offset:%4 sc1 lds"
               ::"s"(lds + (P >> 2) * 4096u), "v"(voff), "s"(srd), "s"(soff + (P >> 2) * 4096u), "i"((P & 3) * 1024) : "memory");
}
template <int OFF> DI void ld128(v4u& d, unsigned voff, srd_t srd, unsigned soff) {
  asm volatile("buffer_load_dwordx4 %0, %1, %2, %3 offen offset:%4 sc1" : "=v"(d) : "v"(voff), "s"(srd), "s"(soff), "i"(OFF) : "memory");
}
template <int OFF> DI void ld128i(v4u& d, unsigned voff, srd_t srd, unsigned soff) {
  asm volatile("buffer_load_dwordx4 %0, %1, %2, %3 offen offset:%4 sc1" : "+v"(d) : "v"(voff), "s"(srd), "s"(soff), "i"(OFF) : "memory");
}
template <int OFF> DI void ld64(v2u& d, unsigned voff, srd_t srd, unsigned soff) {
  asm volatile("buffer_load_dwordx2 %0, %1, %2, %3 offen offset:%4 sc1" : "=v"(d) : "v"(voff), "s"(srd), "s"(soff), "i"(OFF) : "memory");
}
template <int N> DI void wait_tie(v4u& a) { asm volatile("s_waitcnt vmcnt(%1)" : "+v"(a) : "i"(N)); }
template <int N> DI void wait_tie2(v4u& a, v4u& b) { asm volatile("s_waitcnt vmcnt(%2)" : "+v"(a), "+v"(b) : "i"(N)); }
template <int N> DI void wait_tie3(v4u& a, v4u& b, v4u& c) { asm volatile("s_waitcnt vmcnt(%3)" : "+v"(a), "+v"(b), "+v"(c) : "i"(N)); }
DI void tie2(v4u& a, v4u& b) { asm volatile("" : "+v"(a), "+v"(b)); }
DI void tie3(v4u& a, v4u& b, v4u& c) { asm volatile("" : "+v"(a), "+v"(b), "+v"(c)); }
DI void tie1(v4u& a) { asm volatile("" : "+v"(a)); }
DI void st128(v4u v, unsigned voff, srd_t srd, unsigned soff) { __builtin_amdgcn_raw_buffer_store_b128(v, srd, voff, soff, 16); }
DI void st64(v2u v, unsigned voff, srd_t srd, unsigned soff) { __builtin_amdgcn_raw_buffer_store_b64(v, srd, voff, soff, 16); }

DI float asf(unsigned u) { return __builtin_bit_cast(float, u); }
DI unsigned asu(float f) { return __builtin_bit_cast(unsigned, f); }
DI srd_t mk_srd(const void* p, unsigned bytes) { return __builtin_amdgcn_make_buffer_rsrc((void*)p, 0, bytes, 0x00020000); }
DI f32x4 mfma(v4u a, v4u b, f32x4 c) {
  return __builtin_amdgcn_mfma_f32_16x16x32_bf16(__builtin_bit_cast(bf16x8, a), __builtin_bit_cast(bf16x8, b), c, 0, 0, 0);
}
DI void mfma_ip(f32x4& c, const v4u& a, const v4u& b) { asm volatile("v_mfma_f32_16x16x32_bf16 %0, %1, %2, %0" : "+v"(c) : "v"(a), "v"(b)); }
template <int N> DI void acc_fence(f32x4 (&a)[N]) {
  asm volatile("s_nop 15\n\ts_nop 7" ::: "memory");
#pragma unroll
  for (int i = 0; i < N; ++i) asm volatile("" : "+v"(a[i]));
}
template <int N> DI void acc_zero(f32x4 (&a)[N]) {
#pragma unroll
  for (int i = 0; i < N; ++i) { a[i] = f32x4{0.f, 0.f, 0.f, 0.f}; asm volatile("" : "+v"(a[i])); }
  asm volatile("s_nop 1");
}
DI unsigned pk2(float a, float b) { f32x2 v = {a, b}; return __builtin_bit_cast(unsigned, __builtin_convertvector(v, bf2_t)); }
DI unsigned pk2v(f32x2 v) { return __builtin_bit_cast(unsigned, __builtin_convertvector(v, bf2_t)); }
DI float bflo(unsigned u) { return asf(u << 16); }
DI float bfhi(unsigned u) { return asf(u & 0xFFFF0000u); }
DI f32x4 unpk(v2u u) { return f32x4{bflo(u.x), bfhi(u.x), bflo(u.y), bfhi(u.y)}; }
DI v2u pk4(f32x4 v) { return v2u{pk2(v.x, v.y), pk2(v.z, v.w)}; }
DI f32x4 rnd4(f32x4 v) { return unpk(pk4(v)); }
template <int ROW8> DI v4u trfrag(LAS unsigned char* p) {
  s16x4 lo = __builtin_amdgcn_ds_read_tr16_b64_v4i16((LAS s16x4*)p);
  s16x4 hi = __builtin_amdgcn_ds_read_tr16_b64_v4i16((LAS s16x4*)(p + ROW8));
  return __builtin_bit_cast(v4u, __builtin_shufflevector(lo, hi, 0, 1, 2, 3, 4, 5, 6, 7));
}
DI int tr_row(int lane) { const int g = lane >> 4; return 16 * (g >> 1) + 4 * (g & 1) + ((lane >> 2) & 3); }
DI void xr_sum2(float& a, float& b) {
  auto s = __builtin_amdgcn_permlane16_swap(asu(a), asu(b), false, false);
  const float m = asf(s[0]) + asf(s[1]);
  auto t = __builtin_amdgcn_permlane32_swap(asu(m), asu(m), false, false);
  const float n = asf(t[0]) + asf(t[1]);
  auto u = __builtin_amdgcn_permlane16_swap(asu(n), asu(n), false, false);
  a = asf(u[0]); b = asf(u[1]);
}
DI float fmax_raw(float a, float b) { float r; asm("v_max_f32 %0, %1, %2" : "=v"(r) : "v"(a), "v"(b)); return r; }
DI float fmax3_raw(float a, float b, float c) { float r; asm("v_max3_f32 %0, %1, %2, %3" : "=v"(r) : "v"(a), "v"(b), "v"(c)); return r; }
DI float fmin3_raw(float a, float b, float c) { float r; asm("v_min3_f32 %0, %1, %2, %3" : "=v"(r) : "v"(a), "v"(b), "v"(c)); return r; }
DI void xr_max2(float& a, float& b) {
  auto s = __builtin_amdgcn_permlane16_swap(asu(a), asu(b), false, false);
  const float m = __builtin_fmaxf(asf(s[0]), asf(s[1]));
  auto t = __builtin_amdgcn_permlane32_swap(asu(m), asu(m), false, false);
  const float n = __builtin_fmaxf(asf(t[0]), asf(t[1]));
  auto u = __builtin_amdgcn_permlane16_swap(asu(n), asu(n), false, false);
  a = asf(u[0]); b = asf(u[1]);
}
template <bool MAX> DI void xr4(float (&a)[4], float (&b)[4]) {
  float m[4], n[4];
#pragma unroll
  for (int i = 0; i < 4; ++i) { auto s = __builtin_amdgcn_permlane16_swap(asu(a[i]), asu(b[i]), false, false);
                                m[i] = MAX ? fmax_raw(asf(s[0]), asf(s[1])) : asf(s[0]) + asf(s[1]); }
#pragma unroll
  for (int i = 0; i < 4; ++i) { auto t = __builtin_amdgcn_permlane32_swap(asu(m[i]), asu(m[i]), false, false);
                                n[i] = MAX ? fmax_raw(asf(t[0]), asf(t[1])) : asf(t[0]) + asf(t[1]); }
#pragma unroll
  for (int i = 0; i < 4; ++i) { auto u = __builtin_amdgcn_permlane16_swap(asu(n[i]), asu(n[i]), false, false); a[i] = asf(u[0]); b[i] = asf(u[1]); }
}
DI f32x2 gelu2(f32x2 x) {
  constexpr float C0 = -2.0f * 0.7978845608028654f * 1.4426950408889634f, C1 = C0 * 0.044715f;
  const f32x2 a = x * ((x * x) * C1 + C0);
  const f32x2 d = f32x2{__builtin_amdgcn_exp2f(a.x), __builtin_amdgcn_exp2f(a.y)} + 1.0f;
  return x * f32x2{__builtin_amdgcn_rcpf(d.x), __builtin_amdgcn_rcpf(d.y)};
}
DI v2u ln_gelu4(f32x4 acc, f32x4 cs, float nmean, float scale) {
  const f32x2 a = (f32x2{cs.x, cs.y} * nmean + f32x2{acc.x, acc.y}) * scale, b = (f32x2{cs.z, cs.w} * nmean + f32x2{acc.z, acc.w}) * scale;
  return v2u{pk2v(gelu2(a)), pk2v(gelu2(b))};
}
DI void undef5(f32x4 (&a)[5]) { asm volatile("" : "=v"(a[0]), "=v"(a[1]), "=v"(a[2]), "=v"(a[3]), "=v"(a[4])); }
DI void undef5u(v4u (&a)[5]) { asm volatile("" : "=v"(a[0]), "=v"(a[1]), "=v"(a[2]), "=v"(a[3]), "=v"(a[4])); }
DI int opq(int x) { asm volatile("" : "+v"(x)); return x; }
DI int opqs(int x) { asm volatile("" : "+s"(x)); return x; }
template <class T> DI T* opqp(T* p) { asm volatile("" : "+s"(p)); return p; }

struct Ctx {
  unsigned char* ws;
  LAS unsigned char* L;
  int w, r, c, b;
  u64 tag;
  bool dead;
  DI void fresh() { w = opqs(w); r = opqs(r); c = opqs(c); ws = opqp(ws); }
};

DI void stamp(const Ctx&, int) {}
DI void stampc(const Ctx&, int) {}
DI void stamp_dump(const Ctx&) {}
DI void stampw(const Ctx&, int) {}

DI void poll_flags(Ctx& k, unsigned flag_byte_off, u64 want) {
  if (k.w == 7 && !k.dead) {
    GAS u64* p = (GAS u64*)(k.ws + WS_FLAGS + flag_byte_off);
    unsigned spins = 0;
    for (;;) {
      const u64 v = __hip_atomic_load(p, __ATOMIC_RELAXED, __HIP_MEMORY_SCOPE_AGENT);
      if (__all(v == want)) break;
      if (++spins > (1u << 20)) { k.dead = true; __hip_atomic_store((GAS unsigned*)(k.ws + WS_TMO), 1u, __ATOMIC_RELAXED, __HIP_MEMORY_SCOPE_AGENT); break; }
    }
  }
}
DI void poll_flags_any(Ctx& k, unsigned flag_byte_off, u64 want) {
  if (!k.dead) {
    GAS u64* p = (GAS u64*)(k.ws + WS_FLAGS + flag_byte_off);
    unsigned spins = 0;
    for (;;) {
      const u64 v = __hip_atomic_load(p, __ATOMIC_RELAXED, __HIP_MEMORY_SCOPE_AGENT);
      if (__all(v == want)) break;
      if (++spins > (1u << 20)) { k.dead = true; __hip_atomic_store((GAS unsigned*)(k.ws + WS_TMO), 1u, __ATOMIC_RELAXED, __HIP_MEMORY_SCOPE_AGENT); break; }
    }
  }
}
DI v4u granule(f32x4 v, u64 tg) { return v4u{pk2(v.x, v.y), (unsigned)tg, pk2(v.z, v.w), (unsigned)(tg >> 32)}; }
DI bool granule_ok(v4u g, u64 tg) { return g.y == (unsigned)tg && g.w == (unsigned)(tg >> 32); }
DI bool spin_more(Ctx& k, unsigned& spins) {
  if (k.dead) return false;
  __builtin_amdgcn_s_sleep(2);
  if (++spins > (1u << 19)) { k.dead = true; __hip_atomic_store((GAS unsigned*)(k.ws + WS_TMO), 1u, __ATOMIC_RELAXED, __HIP_MEMORY_SCOPE_AGENT); return false; }
  return true;
}
DI void post_flag(const Ctx& k, unsigned flag_byte_off, u64 v) {
  if (threadIdx.x == 7 * 64) __hip_atomic_store((GAS u64*)(k.ws + WS_FLAGS + flag_byte_off), v, __ATOMIC_RELAXED, __HIP_MEMORY_SCOPE_AGENT);
}
DI u64 tag_of(const Ctx& k, int ph) { return k.tag + (u64)(ph + 1); }
DI unsigned fl_group(int ph, int r, int c) { return (unsigned)((ph * 256 + r * 8 + c) * 8); }
DI unsigned fl_col(int ph, int r, int c) { return (unsigned)((ph * 256 + c * 32 + r) * 8); }

constexpr unsigned POISON = 0xFFFFFFFFu;
DI unsigned canon(unsigned u) { return u < POISON - 1u ? u : POISON - 1u; }
DI v2u canon2(v2u a) { return v2u{canon(a.x), canon(a.y)}; }
DI unsigned umax(unsigned a, unsigned b) { return a > b ? a : b; }
DI unsigned umax4(v4u x) { return umax(umax(x.x, x.y), umax(x.z, x.w)); }
DI bool poisoned4(v4u a, v4u b, v4u c, v4u d) { return __any(umax(umax(umax4(a), umax4(b)), umax(umax4(c), umax4(d))) == POISON); }
template <int N> DI void wait_tie4(v4u& a, v4u& b, v4u& c, v4u& d) { asm volatile("s_waitcnt vmcnt(%4)" : "+v"(a), "+v"(b), "+v"(c), "+v"(d) : "i"(N)); }
DI void xcheck4(v4u& a, v4u& b, v4u& c, v4u& d, unsigned vo, srd_t ws, unsigned so) {
  if (__builtin_expect(poisoned4(a, b, c, d), 0)) {
#pragma unroll 1
    for (int spins = 0; spins < (1 << 18); ++spins) {
      if (FINE == 7 && (threadIdx.x & 63) == 0) __hip_atomic_fetch_add((LAS unsigned*)(size_t)(L_MISC + 2816 + 150 * 8), 1u, __ATOMIC_RELAXED, __HIP_MEMORY_SCOPE_WORKGROUP);
      __builtin_amdgcn_s_sleep(1);
      ld128<0>(a, vo, ws, so); ld128<0>(b, vo, ws, so + 4096u); ld128<0>(c, vo, ws, so + 8192u); ld128<0>(d, vo, ws, so + 12288u);
      wait_tie4<0>(a, b, c, d);
      if (!poisoned4(a, b, c, d)) break;
    }
  }
}

template <int NW> DI void wstream(const Ctx& k, srd_t ws, unsigned src, int lp, int np, unsigned v16) {
  if (k.w < NW) {
#pragma unroll 1
    for (int g = k.w * 4; g < np; g += NW * 4) {
      const int s = g + 4 > np ? np - 4 : g;
      gln<4>((unsigned)(lp + s) * 1024u, v16, ws, src + (unsigned)s * 1024u);
    }
  }
}
template <int G> DI void wstream_exact(const Ctx& k, srd_t ws, unsigned src, int lp, int np, unsigned v16) {
  const int g = k.w * G, s = g + G > np ? np - G : g;
  gln<G>((unsigned)(lp + s) * 1024u, v16, ws, src + (unsigned)s * 1024u);
}

struct StatLd { v2u s0, s1; };
DI void stat_issue(StatLd& s, srd_t ws, unsigned statbuf, int tb, int lane) {
  const unsigned vo = (unsigned)((lane >> 4) * 256 + (lane & 15) * 8);
  const unsigned so = WS_STAT + statbuf * STAT_SZ + (unsigned)tb * 1024u;
  ld64<0>(s.s0, vo, ws, so); ld64<128>(s.s1, vo, ws, so);
}
DI void stat_finish(const StatLd& s, float& mean, float& var) {
  float a = asf(s.s0.x) + asf(s.s1.x), q = asf(s.s0.y) + asf(s.s1.y);
  xr_sum2(a, q);
  mean = a * (1.0f / 640.0f);
  var = __builtin_fmaxf(__builtin_fmaf(-mean, mean, q * (1.0f / 640.0f)), 0.0f);
}
template <int N> DI void wait_tie_stat(StatLd& s) { asm volatile("s_waitcnt vmcnt(%2)" : "+v"(s.s0), "+v"(s.s1) : "i"(N)); }

#define MIX(NM, ND) { _Pragma("unroll") for (int m_ = 0; m_ < ND; ++m_) { __builtin_amdgcn_sched_group_barrier(0x008, NM, 0); __builtin_amdgcn_sched_group_barrier(0x100, 1, 0); } }
struct Ring { LAS unsigned char *b0, *b1, *b2; };
DI LAS unsigned char* lds_at(unsigned a) { asm volatile("" : "+v"(a)); return (LAS unsigned char*)(size_t)a; }
DI Ring mk_ring(int lane) { return Ring{lds_at(lane * 16), lds_at(lane * 16 + 12 * SLOT), lds_at(lane * 16 + 24 * SLOT)}; }
template <int N, int BL> DI v4u ring_frag(const Ring& r) {
  constexpr int s = N % 31;
  return *(LAS v4u*)((s < 12 ? r.b0 : s < 24 ? r.b1 : r.b2) + (s % 12) * SLOT + 1024 * BL);
}
template <int N> DI void ring_read(const Ring& r, v4u (&d)[5]) {
  d[0] = ring_frag<N, 0>(r); d[1] = ring_frag<N, 1>(r); d[2] = ring_frag<N, 2>(r); d[3] = ring_frag<N, 3>(r); d[4] = ring_frag<N, 4>(r);
}
template <int N, bool RD> DI void kmma(const Ring& r, f32x4 (&acc)[5], const v4u (&w)[5], const v4u& x, v4u (&nx)[5]) {
  mfma_ip(acc[0], w[0], x); if (RD) nx[0] = ring_frag<N, 0>(r);
  mfma_ip(acc[1], w[1], x); if (RD) nx[1] = ring_frag<N, 1>(r);
  mfma_ip(acc[2], w[2], x); if (RD) nx[2] = ring_frag<N, 2>(r);
  mfma_ip(acc[3], w[3], x); if (RD) nx[3] = ring_frag<N, 3>(r);
  mfma_ip(acc[4], w[4], x); if (RD) nx[4] = ring_frag<N, 4>(r);
}
#define KSTEP(P, NX, NS, i)                                                                   \
  {                                                                                           \
    if (!CHK) wait_tie<kx_wait(i, i, NS)>(xf[i]);                                             \
    else if (i % 4 == 0) {                                                                    \
      wait_tie4<kx_wait(i, i + 3, NS)>(xf[i], xf[(i + 1) % NX], xf[(i + 2) % NX], xf[(i + 3) % NX]); \
      xcheck4(xf[i], xf[(i + 1) % NX], xf[(i + 2) % NX], xf[(i + 3) % NX], v16, ws, xo + i * 4096u); \
    }                                                                                         \
    kmma<P + i + 2, (i + 2 < NX)>(rg, acc, wf[i % 3], xf[i], wf[(i + 2) % 3]);                \
    if (i + X0N < NX) ld128<0>(xf[(i + X0N) % NX], v16, ws, xo + (i + X0N) * 4096u);          \
  }
#define KMAIN(P, NS, CHECK)                                                                   \
  {                                                                                           \
    constexpr bool CHK = CHECK;                                                               \
    v4u wf[3][5];                                                                             \
    ring_read<P>(rg, wf[0]); ring_read<P + 1>(rg, wf[1]);                                     \
    KSTEP(P, 20, NS, 0) KSTEP(P, 20, NS, 1) KSTEP(P, 20, NS, 2) KSTEP(P, 20, NS, 3) KSTEP(P, 20, NS, 4)      \
    KSTEP(P, 20, NS, 5) KSTEP(P, 20, NS, 6) KSTEP(P, 20, NS, 7) KSTEP(P, 20, NS, 8) KSTEP(P, 20, NS, 9)      \
    KSTEP(P, 20, NS, 10) KSTEP(P, 20, NS, 11) KSTEP(P, 20, NS, 12) KSTEP(P, 20, NS, 13) KSTEP(P, 20, NS, 14) \
    KSTEP(P, 20, NS, 15) KSTEP(P, 20, NS, 16) KSTEP(P, 20, NS, 17) KSTEP(P, 20, NS, 18) KSTEP(P, 20, NS, 19) \
    acc_fence(acc);                                                                           \
  }
DI unsigned pin(unsigned x) { asm volatile("" : "+s"(x)); return x; }
DI unsigned pinv(unsigned x) { asm volatile("" : "+v"(x)); return x; }
DI unsigned kx_off(const Ctx& k, unsigned actbuf) { return pin(actbuf + (unsigned)k.r * 81920u + (unsigned)(k.w & 3) * 1024u); }
DI void kx_issue(v4u (&x)[20], srd_t ws, unsigned so, unsigned vo) {
#pragma unroll
  for (int i = 0; i < X0N; ++i) ld128<0>(x[i], vo, ws, so + i * 4096u);
}
constexpr int kx_wait(int i, int j, int ns) { return ((X0N + i - 1 < 19 ? X0N + i - 1 : 19) - j) + (j < X0N ? ns : 0); }
DI void hstream(const Ctx& k, srd_t ws, unsigned src, int lp, int np, unsigned v16) {
  if (k.w >= 4 && k.w < 7) {
#pragma unroll 1
    for (int g = (k.w - 4) * 4; g < np; g += 12) {
      const int s = g + 4 > np ? np - 4 : g;
      gln<4>((unsigned)(lp + s) * 1024u, v16, ws, src + (unsigned)s * 1024u);
    }
  }
}
DI unsigned frag_off(int B, int gq, int t) { return (unsigned)((B >> 1) * 4096 + (2 * (B & 1) + (gq & 1)) * 256 + t * 16 + (gq >> 1) * 8); }
DI unsigned blk_off(int B) { return (unsigned)((B >> 1) * 4096 + (B & 1) * 512); }
DI void kstore(const Ctx& k, srd_t ws, unsigned actbuf, const v2u (&pk)[5], int lane) {
  const unsigned lo = frag_off(0, lane >> 4, lane & 15);
  const unsigned so = actbuf + (unsigned)k.r * 81920u + (unsigned)(k.w & 3) * 1024u;
#pragma unroll
  for (int j = 0; j < 5; ++j) st64(canon2(pk[j]), lo, ws, so + blk_off(5 * k.c + j));
}
DI void kstats(const Ctx& k, srd_t ws, unsigned statbuf, const f32x4 (&v)[5], int lane) {
  f32x2 s = {0.f, 0.f}, q = {0.f, 0.f};
#pragma unroll
  for (int j = 0; j < 5; ++j) {
    const f32x2 a = {v[j].x, v[j].y}, b = {v[j].z, v[j].w};
    s += a + b; q = a * a + q; q = b * b + q;
  }
  float s1 = s.x + s.y, q1 = q.x + q.y;
  xr_sum2(s1, q1);
  const unsigned vo = lane < 16 ? (unsigned)(lane * 8) : OOB;
  st64(v2u{canon(asu(s1)), canon(asu(q1))}, vo, ws, WS_STAT + statbuf * STAT_SZ + (unsigned)((k.r * 4 + (k.w & 3)) * 1024 + k.c * 128));
}
DI void poison_tb(const Ctx& k, srd_t ws, int ly, int tb, int lane) {
  const int r = k.r, c = k.c, t = lane & 15, g = lane >> 4;
  const unsigned act = WS_ACT + (unsigned)ly * ACT_L;
  const v2u p2 = {POISON, POISON};
  const unsigned lo = frag_off(0, g, t);
  const int shift = (c >> 1) == 0 ? 1 : (c >> 1) == 1 ? -1 : (c >> 1) == 2 ? 2 : -2;
  const int tok = r * 64 + tb * 16 + t, dst = tok + shift;
  const unsigned dof = (dst >= 0 && dst < S_) ? (unsigned)(dst >> 6) * 81920u + (unsigned)((dst >> 4) & 3) * 1024u + frag_off(0, g, dst & 15) : OOB;
  const bool z = shift > 0 ? tok < shift : tok >= S_ + shift;
  const unsigned zof = z ? (unsigned)(tok >> 6) * 81920u + (unsigned)((tok >> 4) & 3) * 1024u + lo : OOB;
#pragma unroll
  for (int j = 0; j < 5; ++j) {
    st64(p2, lo, ws, act + A_O + (unsigned)r * 81920u + tb * 1024u + blk_off(5 * c + j));
    st64(p2, lo, ws, act + A_X2 + (unsigned)r * 81920u + tb * 1024u + blk_off(5 * c + j));
    st64(p2, dof, ws, act + A_HS + blk_off(5 * c + j));
    if (r == 0 || r == 31) st64(p2, zof, ws, act + A_HS + blk_off(5 * c + j));
  }
  st64(p2, lane < 16 ? (unsigned)(lane * 8) : OOB, ws, WS_STAT + (unsigned)(ly * 3) * STAT_SZ + (unsigned)((r * 4 + tb) * 1024 + c * 128));
#pragma unroll
  for (int u = 0; u < 6; ++u)
    st128(v4u{POISON, POISON, POISON, POISON}, lane * 16, ws, act + A_H2 + (unsigned)r * 196608u + (unsigned)(24 * c + 6 * tb + u) * 1024u);
}
DI void chunk_sum(v4u f, srd_t ws, int c, int s, int fi, int lane) {
  const v4u ones = {0x3F803F80u, 0x3F803F80u, 0x3F803F80u, 0x3F803F80u};
  const f32x4 cs = mfma(f, ones, f32x4{0.f, 0.f, 0.f, 0.f});
  st128(__builtin_bit_cast(v4u, cs), (lane & 15) == 0 ? (unsigned)(lane >> 4) * 16u : OOB, ws, WS_PCS + (unsigned)c * PCS_C + (unsigned)(s * 33 + fi) * 64u);
}

__global__ void __launch_bounds__(512, 2)
hedgehog_smlp_kernel(const unsigned char* x, const unsigned char* wqkv, const unsigned char* wphi, const unsigned char* wo,
                     const unsigned char* w1, const unsigned char* w2, const unsigned char* w3, const unsigned char* w4,
                     unsigned char* out, unsigned char* wsp) {
  extern __shared__ __attribute__((aligned(16))) unsigned char smem[];
  Ctx k;
  k.ws = wsp;
  k.L = (LAS unsigned char*)smem;
  const int tid = threadIdx.x;
  const int lane0 = tid & 63;
  k.w = __builtin_amdgcn_readfirstlane(tid >> 6);
  k.b = blockIdx.x;
#define MAPMODE 2
  { const int xc = k.b & 7, i = k.b >> 3;
    if (MAPMODE == 0) { k.r = xc * 4 + (i >> 3); k.c = i & 7; }
    else if (MAPMODE == 2) { k.r = (xc >> 1) * 8 + (i >> 2); k.c = (xc & 1) * 4 + (i & 3); }
    else { k.r = (xc >> 2) * 16 + (i >> 1); k.c = (xc & 3) * 2 + (i & 1); } }
  k.dead = false;
  f32x4 xres[5];
  v4u aphi[2];
#define PHASE_VARS()                                                             \
  k.fresh();                                                                     \
  const int w = k.w, r = k.r, c = k.c;                                           \
  const srd_t ws = mk_srd(k.ws, WS_END);                                         \
  const int ly = opqs(layer), ph0 = ly * PH_PER_LAYER, sb = 2 + ly * 64;         \
  const unsigned act = WS_ACT + (unsigned)ly * ACT_L;                            \
  const unsigned wt = WS_WT + (unsigned)c * WT_C;                                \
  (void)w; (void)r; (void)c; (void)ph0; (void)sb; (void)act; (void)wt;
  stamp(k, 0); stampc(k, 126);
#define CODEPF 1
  constexpr int CODE_KB = 38;
  v4u code_sink = {0u, 0u, 0u, 0u};
  if (CODEPF) {
    unsigned long long pc;
    asm volatile("s_getpc_b64 %0" : "=s"(pc));
    const unsigned long long a = pc + (unsigned long long)((((k.b >> 3) * 8 + k.w) % CODE_KB) * 1024 + lane0 * 16);
    asm volatile("global_load_dwordx4 %0, %1, off" : "=v"(code_sink) : "v"(a) : "memory");
  }
  {
    const int w = k.w, r = k.r, c = k.c;
    const srd_t ws = mk_srd(wsp, WS_END);
    const unsigned wt = WS_WT + (unsigned)c * WT_C;
    const int lane = opq(lane0), g = lane >> 4, p = lane & 3, trr = tr_row(lane);
    if (r < 20) {
      const srd_t srd = mk_srd(wqkv + 160 * c + (size_t)r * 122880, 122880 - 160 * c);
#pragma unroll
      for (int u = 0; u < 2; ++u) {
        const unsigned id = (2 * w + u) * 64 + lane, row = (id * 2185u) >> 16, rem = id - row * 30u, sec = (rem * 6554u) >> 16;
        if (2 * w + u < 15) glds(L_S0 + (2 * w + u) * 1024u, row * 3840u + sec * 1280u + (rem - sec * 10u) * 16u, srd, 0);
      }
    }
    const int J = r >= 20 ? (r - 20) * 8 + w : 96 + r * 8 + w;
    const unsigned sj = L_SJ + w * 6144u;
    if (J < 108) {
      const unsigned char* W = J < 20 ? wo : J < 40 ? w1 : J < 60 ? w2 : w4;
      const int s = J < 20 ? J : J < 40 ? J - 20 : J < 60 ? J - 40 : J - 60;
      const srd_t srd = mk_srd(W + 160 * c + (size_t)s * 40960, 40960 - 160 * c);
#pragma unroll
      for (int j = 0; j < 5; ++j) {
        const unsigned t = 64 * j + lane, row = (t * 6554u) >> 16;
        glds(sj + j * 1024u, row * 1280u + (t - row * 10u) * 16u, srd, 0);
      }
    } else if (J < 148) {
      const int s = (J - 108) >> 1, half = (J - 108) & 1;
      const srd_t srd = mk_srd(w3 + 384 * c + 192 * half + (size_t)s * 98304, 98304 - 384 * c - 192 * half);
#pragma unroll
      for (int j = 0; j < 6; ++j) {
        const unsigned t = 64 * j + lane, row = (t * 5462u) >> 16;
        glds(sj + j * 1024u, row * 3072u + (t - row * 12u) * 16u, srd, 0);
      }
    }
    if (FINE == 6) stamp(k, 121);
    {
      u64 e = __hip_atomic_load((GAS u64*)(wsp + WS_EPOCH), __ATOMIC_RELAXED, __HIP_MEMORY_SCOPE_AGENT);
      const unsigned lo = __builtin_amdgcn_readfirstlane((unsigned)e), hi = __builtin_amdgcn_readfirstlane((unsigned)(e >> 32));
      k.tag = ((u64)hi << 32) | lo;
    }
    v4u xin0[10];
    {
      const GAS v4u* px = (const GAS v4u*)(x + (size_t)(r * 64 + (tid >> 3)) * 1280 + (tid & 7) * 16);
#pragma unroll
      for (int i = 0; i < 10; ++i) xin0[i] = px[8 * i];
    }
    if (w == 7) {
      const srd_t srd = mk_srd(wphi + 3200 * c, 3200);
#pragma unroll
      for (int u = 0; u < 4; ++u) glds(L_SPH + u * 1024u, lane * 16 + u * 1024, srd, 0);
    }
    if (FINE == 6) { stamp(k, 122); WAIT_VM(0); stampw(k, 128); }
    if (FINE == 7 && tid == 0) *(LAS u64*)(k.L + L_MISC + 2816 + 150 * 8) = 0;
    BAR_VL0();
    asm volatile("" ::"v"(code_sink));
    stamp(k, 120);
    {
      const int row = lane & 15;
      LAS unsigned short* pw = (LAS unsigned short*)(k.L + L_SPH) + (w & 3) * 400;
#pragma unroll
      for (int fb = 0; fb < 2; ++fb) {
        const bool rowok = fb == 0 || (row & 3) == 0;
        const int f = 5 * (row >> 2) + (fb == 0 ? (row & 3) : 4);
        unsigned e[8];
#pragma unroll
        for (int j = 0; j < 8; ++j) {
          const bool ok = rowok && (j < 4 || g == 0);
          const int d = j < 4 ? 4 * g + j : 16 + (j - 4);
          const unsigned v = pw[(ok ? d : 0) * 20 + f];
          e[j] = ok ? v : 0u;
        }
        aphi[fb] = v4u{e[0] | (e[1] << 16), e[2] | (e[3] << 16), e[4] | (e[5] << 16), e[6] | (e[7] << 16)};
      }
    }
    if (r < 20) {
      const int j = w >> 1, role = w & 1, vqp = role == 0 ? p : (p == 1 ? 0 : 4);
      const v4u f0 = trfrag<3840>(k.L + L_S0 + trr * 480 + role * 160 + 40 * j + 8 * p);
      const v4u f1 = trfrag<3840>(k.L + L_S0 + trr * 480 + 40 * j + (p == 0 ? role * 160 + 32 : 320 + 8 * vqp));
      st128(f0, lane * 16, ws, wt + WT_A + r * 16384u + w * 2048u);
      st128(f1, lane * 16, ws, wt + WT_A + r * 16384u + w * 2048u + 1024u);
      chunk_sum(f0, ws, c, r, 2 * w, lane); chunk_sum(f1, ws, c, r, 2 * w + 1, lane);
    }
    if (J < 108) {
      const int s = J < 20 ? J : J < 40 ? J - 20 : J < 60 ? J - 40 : J - 60;
      const int n = s;
      const unsigned dst = wt + (J < 20 ? WT_O : J < 40 ? WT_1 : J < 60 ? WT_2 : WT_4) + n * 5120u;
#pragma unroll
      for (int bl = 0; bl < 5; ++bl) {
        const v4u f = trfrag<1280>(k.L + sj + trr * 160 + p * 8 + 32 * bl);
        st128(f, lane * 16, ws, dst + bl * 1024u);
        if (J >= 20 && J < 40) chunk_sum(f, ws, c, s, 16 + bl, lane);
      }
    } else if (J < 148) {
      const int s = (J - 108) >> 1, half = (J - 108) & 1;
#pragma unroll
      for (int bl = 0; bl < 6; ++bl) {
        const v4u f = trfrag<1536>(k.L + sj + trr * 192 + p * 8 + 32 * bl);
        st128(f, lane * 16, ws, wt + WT_3 + s * 12288u + (6 * half + bl) * 1024u);
        chunk_sum(f, ws, c, s, 21 + 6 * half + bl, lane);
      }
    }
    BAR_VL0();
    post_flag(k, fl_col(PH_T, r, c), tag_of(k, PH_T));
    if (w >= 4) poison_tb(k, ws, 0, w - 4, lane);
    if (FINE == 6) stamp(k, 123);
    {
      const int tok = tid >> 3, part = tid & 7;
      float s = 0.f, q = 0.f;
#pragma unroll
      for (int i = 0; i < 10; ++i) {
        const int u = part + 8 * i;
        const int B = u >> 1, gq = (u & 1) * 2;
        LAS unsigned char* q0 = k.L + L_X + ((B >> 1) * 4 + (tok >> 4)) * 1024 + (tok & 15) * 16 + (B & 1) * 512 + (gq >> 1) * 8;
        *(LAS v2u*)(q0) = v2u{xin0[i].x, xin0[i].y};
        *(LAS v2u*)(q0 + 256) = v2u{xin0[i].z, xin0[i].w};
#pragma unroll
        for (int e = 0; e < 4; ++e) {
          const float a = bflo(xin0[i][e]), bb = bfhi(xin0[i][e]);
          s += a + bb; q = __builtin_fmaf(a, a, q); q = __builtin_fmaf(bb, bb, q);
        }
      }
      s += __shfl_xor(s, 1); q += __shfl_xor(q, 1); s += __shfl_xor(s, 2); q += __shfl_xor(q, 2); s += __shfl_xor(s, 4); q += __shfl_xor(q, 4);
      const float mean = s * (1.0f / 640.0f), var = __builtin_fmaxf(__builtin_fmaf(-mean, mean, q * (1.0f / 640.0f)), 0.0f);
      if (part == 0) *(LAS f32x2*)(k.L + L_STATS + tok * 8) = f32x2{mean, __builtin_amdgcn_rsqf(var + 1e-5f)};
    }
    BAR_L();
#pragma unroll
    for (int j = 0; j < 5; ++j) {
      const int B = 5 * c + j;
      xres[j] = unpk(*(LAS v2u*)(k.L + L_X + ((B >> 1) * 4 + (w & 3)) * 1024 + (2 * (B & 1) + (g & 1)) * 256 + (lane & 15) * 16 + (g >> 1) * 8));
    }
  }
  stamp(k, 1);

  f32x4 x2r[5], x1r[5];

#pragma unroll 1
  for (int layer = 0; layer < 2; ++layer) {
    {
      PHASE_VARS()
      const int lane = opq(lane0), t = lane & 15, g = lane >> 4, j = w & 3, role = w < 4 ? 1 : 0;
      const unsigned v16 = pinv(lane * 16);
      const unsigned wa = pin(wt + WT_A + (2 * j + role) * 2048u);
#define A_KS(i) (i)
#define A_WLOAD(i) { ld128<0>(wr[(i) % 5][0], v16, ws, wa + A_KS(i) * 16384u); ld128<1024>(wr[(i) % 5][1], v16, ws, wa + A_KS(i) * 16384u); }
#define A_WLOADI(i) { ld128i<0>(wr[i][0], v16, ws, wa + (i) * 16384u); ld128i<1024>(wr[i][1], v16, ws, wa + (i) * 16384u); }
#define A_WINDOW() { A_WLOADI(0) A_WLOADI(1) }
      const unsigned xa = pin((unsigned)(w < 4 ? w : w + 6) * 4096u), xb2 = pin((unsigned)(w < 4 ? 8 + (w >> 1) : 16 + (w >> 1)) * 4096u + (w & 1) * 2048u);
      const unsigned xsrc = pin(WS_ACT + A_XO + (unsigned)r * 81920u);
      v4u wr[5][2];
#pragma unroll
      for (int i = 0; i < 5; ++i) asm volatile("" : "=v"(wr[i][0]), "=v"(wr[i][1]));
      const bool early = ly != 0 && w != 7;
      if (early) A_WINDOW()
      if (ly == 0) poll_flags(k, fl_col(PH_T, lane & 31, c), tag_of(k, PH_T));
      else poll_flags(k, fl_group(PH_XO, r, lane & 7), tag_of(k, PH_XO));
      BAR();
      FST(42)
      if (ly != 0) { gln<4>(L_X + xa, v16, ws, xsrc + xa); gln<4>(L_X + xa + 16384u, v16, ws, xsrc + xa + 16384u); gln<2>(L_X + xb2, v16, ws, xsrc + xb2); }
      if (!early) A_WINDOW()
      if (ly == 0) {
        if (tid < 132) {
          v4u pc[20];
#pragma unroll
          for (int s2 = 0; s2 < 20; ++s2) pc[s2] = __builtin_amdgcn_raw_buffer_load_b128(ws, tid * 16, WS_PCS + (unsigned)c * PCS_C + s2 * 33 * 64, 16);
          SB();
          f32x4 a = {0.f, 0.f, 0.f, 0.f};
#pragma unroll
          for (int s2 = 0; s2 < 20; ++s2) a += __builtin_bit_cast(f32x4, pc[s2]);
          *(LAS f32x4*)(k.L + L_CSL + tid * 16) = a;
        }
        if (FINE == 6) stamp(k, 112);
        BAR_VL0();
        post_flag(k, fl_group(PH_TG, r, c), tag_of(k, PH_TG));
      } else {
        if (w < 4) WAIT_VM(6); else if (w < 7) WAIT_VM(10);
        BAR();
      }
      StatLd st;
      A_WLOAD(2) A_WLOAD(3) A_WLOAD(4)
      stamp(k, sb + 0);
      f32x4 acc[2][4];
      acc_zero(acc[0]); acc_zero(acc[1]);
      v4u xf[2][4], xn[4];
      LAS unsigned char *xb = lds_at(L_X + lane * 16), *xb2l = lds_at(L_X + 65536 + lane * 16);
#define A_READ(i) { _Pragma("unroll") for (int tb = 0; tb < 4; ++tb) xf[(i) & 1][tb] = *(LAS v4u*)(xb + ((i) * 4 + tb) * 1024); }
#define A_IT(i, N)                                                                                    \
  {                                                                                                   \
    wait_tie2<N>(wr[(i) % 5][0], wr[(i) % 5][1]);                                                     \
    if ((i) == 3) BAR();                    \
    _Pragma("unroll") for (int tb = 0; tb < 4; ++tb) {                                                \
      mfma_ip(acc[0][tb], wr[(i) % 5][0], xf[(i) & 1][tb]);                                           \
      mfma_ip(acc[1][tb], wr[(i) % 5][1], xf[(i) & 1][tb]);                                           \
      if ((i) + 1 < 20) xn[tb] = *(LAS v4u*)(((i) + 1 < 16 ? xb : xb2l) + (((i) + 1) % 16 * 4 + tb) * 1024); \
    }                                                                                                 \
    if ((i) + 1 < 20) { _Pragma("unroll") for (int tb = 0; tb < 4; ++tb) xf[((i) + 1) & 1][tb] = xn[tb]; } \
    if ((i) + 5 < 20) A_WLOAD((i) + 5)                                                                \
    if ((i) == 1) stat_issue(st, ws, 2, r * 4 + (w & 3), lane);          \
    if ((i) >= 12) gl1<((i) >= 12 ? (i) - 12 : 0)>(wol, v16, ws, wog);        \
  }
      const unsigned woa = (unsigned)(8 * w < 52 ? 8 * w : 52) * 1024u, wol = pin((P_C * 5 + 40) * 1024u + woa), wog = pin(wt + WT_O + 40u * 1024u + woa);
      FST(20)
      A_READ(0)
#define A_W7() if (w == 7) WAIT_VM(8);
      A_W7() A_IT(0, 63) A_W7() A_IT(1, 63) A_IT(2, 10) A_IT(3, 10) FST(21) A_IT(4, 10) A_IT(5, 10) A_IT(6, 10) A_IT(7, 8) FST(22)
      A_IT(8, 8) A_IT(9, 8) A_IT(10, 8) A_IT(11, 8) FST(23) A_IT(12, 8) A_IT(13, 9) A_IT(14, 10) A_IT(15, 11) FST(24)
      A_IT(16, 10) A_IT(17, 9) A_IT(18, 7) A_IT(19, 5)
#undef A_IT
#undef A_W7
#undef A_READ
#undef A_WINDOW
#undef A_WLOADI
#undef A_WLOAD
#undef A_KS
      acc_fence(acc[0]); acc_fence(acc[1]);
      if (ly) {
        wait_tie_stat<63>(st);
        float mean, var; stat_finish(st, mean, var);
        if (lane < 16) *(LAS f32x2*)(k.L + L_STATS + ((w & 3) * 16 + lane) * 8) = f32x2{mean, __builtin_amdgcn_rsqf(var + 1e-5f)};
      }
      BAR_L();
      stamp(k, sb + 1);
      const f32x4 c0 = *(LAS f32x4*)(k.L + L_CSL + (2 * (2 * j + role)) * 64 + g * 16), c1 = *(LAS f32x4*)(k.L + L_CSL + (2 * (2 * j + role) + 1) * 64 + g * 16);
      unsigned phq[4][5];
      LAS unsigned char* img = k.L + L_IMG + j * IMG_H;
      {
        v4u bfr[4];
        f32x2 ms[4];
#pragma unroll
        for (int tb = 0; tb < 4; ++tb) ms[tb] = *(LAS f32x2*)(k.L + L_STATS + (tb * 16 + t) * 8);
#pragma unroll
        for (int tb = 0; tb < 4; ++tb) {
          const float nm = -ms[tb].x, rs = ms[tb].y;
          const f32x4 a0 = acc[0][tb], a1 = acc[1][tb];
          bfr[tb] = v4u{pk2v((f32x2{c0.x, c0.y} * nm + f32x2{a0.x, a0.y}) * rs), pk2v((f32x2{c0.z, c0.w} * nm + f32x2{a0.z, a0.w}) * rs),
                        pk2v((f32x2{c1.x, c1.y} * nm + f32x2{a1.x, a1.y}) * rs), pk2v((f32x2{c1.z, c1.w} * nm + f32x2{a1.z, a1.w}) * rs)};
        }
        f32x4 pa[4], pb[4];
        const f32x4 z = {0.f, 0.f, 0.f, 0.f};
#pragma unroll
        for (int tb = 0; tb < 4; ++tb) { pa[tb] = mfma(aphi[0], bfr[tb], z); pb[tb] = mfma(aphi[1], bfr[tb], z); }
        {
          const int vslot = role == 0 ? (g == 0 ? 6 : g) : (g == 0 ? 7 : g == 1 ? 0 : g == 2 ? 4 : 5);
          const bool ones = role == 1 && g == 3;
          LAS unsigned char* vrow = img + t * 160 + 96 + 8 * vslot;
#pragma unroll
          for (int tb = 0; tb < 4; ++tb) *(LAS v2u*)(vrow + tb * 2560) = ones ? v2u{0x00003F80u, 0u} : v2u{bfr[tb].z, bfr[tb].w};
        }
        BAR_L();
        constexpr float L2E = 1.4426950408889634f;
        f32x2 p01[4], p23[4]; float p4[4], mx[4], mn[4];
#pragma unroll
        for (int tb = 0; tb < 4; ++tb) {
          p01[tb] = f32x2{pa[tb].x, pa[tb].y} * L2E; p23[tb] = f32x2{pa[tb].z, pa[tb].w} * L2E; p4[tb] = pb[tb].x * L2E;
          mx[tb] = fmax3_raw(fmax3_raw(p01[tb].x, p01[tb].y, p23[tb].x), p23[tb].y, p4[tb]);
          mn[tb] = -fmin3_raw(fmin3_raw(p01[tb].x, p01[tb].y, p23[tb].x), p23[tb].y, p4[tb]);
        }
        xr4<true>(mx, mn);
        SB();
        float e[4][5], f[4][5], sp[4], sm[4];
#pragma unroll
        for (int tb = 0; tb < 4; ++tb) {
          const f32x2 a = p01[tb] - mx[tb], b = p23[tb] - mx[tb], cc = p01[tb] + mn[tb], d = p23[tb] + mn[tb];
          e[tb][0] = __builtin_amdgcn_exp2f(a.x); e[tb][1] = __builtin_amdgcn_exp2f(a.y); e[tb][2] = __builtin_amdgcn_exp2f(b.x);
          e[tb][3] = __builtin_amdgcn_exp2f(b.y); e[tb][4] = __builtin_amdgcn_exp2f(p4[tb] - mx[tb]);
          f[tb][0] = __builtin_amdgcn_exp2f(-cc.x); f[tb][1] = __builtin_amdgcn_exp2f(-cc.y); f[tb][2] = __builtin_amdgcn_exp2f(-d.x);
          f[tb][3] = __builtin_amdgcn_exp2f(-d.y); f[tb][4] = __builtin_amdgcn_exp2f(-(p4[tb] + mn[tb]));
        }
#pragma unroll
        for (int tb = 0; tb < 4; ++tb) {
          const f32x2 s2 = (f32x2{e[tb][0], e[tb][1]} + f32x2{e[tb][2], e[tb][3]}), s3 = (f32x2{f[tb][0], f[tb][1]} + f32x2{f[tb][2], f[tb][3]});
          sp[tb] = (s2.x + s2.y) + e[tb][4]; sm[tb] = (s3.x + s3.y) + f[tb][4];
        }
        xr4<false>(sp, sm);
        SB();
#pragma unroll
        for (int tb = 0; tb < 4; ++tb) {
          const float rp = __builtin_amdgcn_rcpf(sp[tb]), rm = __builtin_amdgcn_rcpf(sm[tb]);
          phq[tb][0] = pk2v(f32x2{e[tb][0], e[tb][1]} * rp); phq[tb][1] = pk2v(f32x2{e[tb][2], e[tb][3]} * rp);
          phq[tb][2] = pk2(e[tb][4] * rp, f[tb][0] * rm);
          phq[tb][3] = pk2v(f32x2{f[tb][1], f[tb][2]} * rm); phq[tb][4] = pk2v(f32x2{f[tb][3], f[tb][4]} * rm);
        }
        if (role == 1) {
          LAS unsigned char* row = img + t * 160 + 24 * g;
#pragma unroll
          for (int tb = 0; tb < 4; ++tb) {
            *(LAS v2u*)(row + tb * 2560) = v2u{phq[tb][0], phq[tb][1]};
            *(LAS v2u*)(row + tb * 2560 + 8) = v2u{phq[tb][2], phq[tb][3]};
            *(LAS v2u*)(row + tb * 2560 + 16) = v2u{phq[tb][4], 0u};
          }
        }
      }
      FST(25)
      if (role == 1) {
        asm volatile("s_waitcnt lgkmcnt(0)" ::: "memory");
        const unsigned lb = tr_row(lane) * 160 + (lane & 3) * 8;
        f32x4 kv[2][3];
#pragma unroll
        for (int cb = 0; cb < 3; ++cb) { kv[0][cb] = f32x4{0.f, 0.f, 0.f, 0.f}; kv[1][cb] = f32x4{0.f, 0.f, 0.f, 0.f}; }
#pragma unroll
        for (int ks = 0; ks < 2; ++ks) {
          const v4u bv0 = trfrag<1280>(img + lb + ks * 5120 + 96), bv1 = trfrag<1280>(img + lb + ks * 5120 + 128);
#pragma unroll
          for (int cb = 0; cb < 3; ++cb) {
            const v4u a = trfrag<1280>(img + lb + ks * 5120 + 32 * cb);
            kv[0][cb] = mfma(a, bv0, kv[0][cb]); kv[1][cb] = mfma(a, bv1, kv[1][cb]);
          }
        }
        const unsigned so = WS_PART + ly * PART_L + (unsigned)(c * 32 + r) * 16384u + j * 4096u;
        const unsigned vo2 = t < 5 ? (unsigned)(3072 + (g * 5 + t) * 16) : OOB;
        const u64 tg = tag_of(k, ph0 + PH_KV1);
#pragma unroll
        for (int cb = 0; cb < 3; ++cb) { st128(granule(kv[0][cb], tg), v16, ws, so + cb * 1024u); st128(granule(kv[1][cb], tg), vo2, ws, so + cb * 320u); }
      }
      FST(26)
      stamp(k, sb + 2);
      if (w == 1 && ly == 0) poll_flags_any(k, fl_group(PH_TG, r, lane & 7), tag_of(k, PH_TG));
      if (w >= 1 && w < 4) {
#pragma unroll 1
        for (int g4 = (w - 1) * 4; g4 < 40; g4 += 12) gln<4>((unsigned)(P_C * 5 + g4) * 1024u, v16, ws, wt + WT_O + (unsigned)g4 * 1024u);
      }
      if (w == 0) {
        const unsigned so = WS_PART + ly * PART_L + (unsigned)(c * 32) * 16384u + r * 512u;
        const unsigned vo2 = (unsigned)((lane >> 5) * 16384 + (lane & 31) * 16);
        const bool hole = (r & 7) == 7 && (lane & 31) >= 28;
        const u64 tg = tag_of(k, ph0 + PH_KV1);
        v4u pr[16];
        if (FINE == 4 && ly == 1) stampw(k, 128);
        unsigned spins = 0;
        for (;;) {
          bool ok = true;
#pragma unroll
          for (int i = 0; i < 16; ++i) pr[i] = __builtin_amdgcn_raw_buffer_load_b128(ws, vo2, so + i * 32768u, 16);
          SB();
#pragma unroll
          for (int i = 0; i < 16; ++i) ok = ok && granule_ok(pr[i], tg);
          if (__all(ok || hole) || !spin_more(k, spins)) break;
        }
        if (FINE == 4 && ly == 1) { stampw(k, 136); if (lane == 0) *(LAS u64*)(k.L + L_MISC + 2816 + 152 * 8) = spins; }
        f32x4 sum = {0.f, 0.f, 0.f, 0.f};
#pragma unroll
        for (int i = 0; i < 16; ++i) sum += f32x4{bflo(pr[i].x), bfhi(pr[i].x), bflo(pr[i].z), bfhi(pr[i].z)};
#pragma unroll
        for (int e = 0; e < 4; ++e) {
          auto sw = __builtin_amdgcn_permlane32_swap(asu(sum[e]), asu(sum[e]), false, false);
          sum[e] = asf(sw[0]) + asf(sw[1]);
        }
        st128(granule(sum, tag_of(k, ph0 + PH_KV2)), lane < 32 ? v16 : OOB, ws, WS_RED + (unsigned)(ly * 8 + c) * 16384u + r * 512u);
        stamp(k, sb + 3);
      } else if (role == 0) {
        const unsigned so = WS_RED + (unsigned)(ly * 8 + c) * 16384u + j * 4096u;
        const int dd = t < 4 ? t : ((t & 3) == 0 ? 4 : -1);
        const int Q0 = 20 * c + 5 * j + g, Q1 = 20 * c + 5 * j + 4;
        const unsigned o0 = pinv(frag_off(Q0 >> 2, Q0 & 3, t)), o1 = pinv(g == 0 ? frag_off(Q1 >> 2, Q1 & 3, t) : OOB);
        const u64 tg = tag_of(k, ph0 + PH_KV2);
        v4u l0[3], l1[3];
        if (FINE == 4 && ly == 1) stampw(k, 128);
        unsigned spins = 0;
        for (;;) {
          bool ok = true;
#pragma unroll
          for (int u = 0; u < 3; ++u) {
            const int qc = 3 * g + u;
            l0[u] = __builtin_amdgcn_raw_buffer_load_b128(ws, (unsigned)((qc >> 2) * 1024 + ((qc & 3) * 16 + t) * 16), so, 16);
            l1[u] = __builtin_amdgcn_raw_buffer_load_b128(ws, dd >= 0 ? (unsigned)(3072 + (qc * 5 + dd) * 16) : OOB, so, 16);
          }
          SB();
#pragma unroll
          for (int u = 0; u < 3; ++u) ok = ok && granule_ok(l0[u], tg) && (dd < 0 || granule_ok(l1[u], tg));
          if (__all(ok) || !spin_more(k, spins)) break;
        }
        if (FINE == 4 && ly == 1) { stampw(k, 136); if (lane == 0 && w == 0) *(LAS u64*)(k.L + L_MISC + 2816 + 153 * 8) = spins; }
        stamp(k, sb + 3);
        const v4u a00 = {l0[0].x, l0[0].z, l0[1].x, l0[1].z}, a01 = {l0[2].x, l0[2].z, 0u, 0u};
        const v4u a10 = {l1[0].x, l1[0].z, l1[1].x, l1[1].z}, a11 = {l1[2].x, l1[2].z, 0u, 0u};
        f32x4 n0[4], n1[4];
        const f32x4 z = {0.f, 0.f, 0.f, 0.f};
#pragma unroll
        for (int tb = 0; tb < 4; ++tb) {
          const v4u b0 = {phq[tb][0], phq[tb][1], phq[tb][2], phq[tb][3]}, b1 = {phq[tb][4], 0u, 0u, 0u};
          n0[tb] = mfma(a01, b1, mfma(a00, b0, z)); n1[tb] = mfma(a11, b1, mfma(a10, b0, z));
        }
#pragma unroll
        for (int tb = 0; tb < 4; ++tb) {
          auto sw = __builtin_amdgcn_permlane16_swap(asu(n1[tb].x), asu(n1[tb].x), false, false);
          const float rd = __builtin_amdgcn_rcpf((g == 0 ? asf(sw[1]) : n1[tb].x) + 1e-6f);
          const unsigned so2 = act + A_O + (unsigned)r * 81920u + tb * 1024u;
          st64(canon2(pk4(n0[tb] * rd)), o0, ws, so2);
          st64(canon2(pk4(n1[tb] * rd)), o1, ws, so2);
        }
      }
      if (w >= 1 && w < 4) WAIT_VM(0);
      BAR();
      stamp(k, sb + 4);
    }
    if (STOP_PHASE == layer * 7 + 0) return;
    {
      PHASE_VARS()
      const int lane = opq(lane0);
      const unsigned v16 = pinv(lane * 16), xo = kx_off(k, act + A_O);
      const Ring rg = mk_ring(lane);
      f32x4 acc[5];
      if (w < 4) {
        v4u xf[20];
        __builtin_amdgcn_s_sleep(XDELAY);
        kx_issue(xf, ws, xo, v16);
        stamp(k, sb + 5);
        acc_zero(acc);
        KMAIN(P_C, 0, true)
      } else {
        hstream(k, ws, wt + WT_1, P_D * 5, 15, v16);
        hstream(k, ws, wt + WT_1 + 15 * 1024, 0, 40, v16);
        if (ly == 0) poison_tb(k, ws, 1, w - 4, lane);
        undef5(acc);
      }
      BAR_L();
      stamp(k, sb + 6);
      if (w < 4) {
        v2u pk[5];
#pragma unroll
        for (int j = 0; j < 5; ++j) { pk[j] = pk4(xres[j] + rnd4(acc[j])); x2r[j] = unpk(pk[j]); }
        kstore(k, ws, act + A_X2, pk, lane);
        kstats(k, ws, ly * 3 + 0, x2r, lane);
        FST(28)
      } else {
        hstream(k, ws, wt + WT_1 + 55 * 1024, 40, 45, v16);
        undef5(x2r);
      }
      stamp(k, sb + 7);
    }
    if (STOP_PHASE == layer * 7 + 1) return;
    {
      PHASE_VARS()
      const int lane = opq(lane0), t = lane & 15, g = lane >> 4;
      const unsigned v16 = pinv(lane * 16), xo = kx_off(k, act + A_X2);
      const Ring rg = mk_ring(lane);
      const int shift = (c >> 1) == 0 ? 1 : (c >> 1) == 1 ? -1 : (c >> 1) == 2 ? 2 : -2;
      const int tok = r * 64 + (w & 3) * 16 + t, dst = tok + shift;
      const unsigned dof = pinv((dst >= 0 && dst < S_) ? (unsigned)(dst >> 6) * 81920u + (unsigned)((dst >> 4) & 3) * 1024u + frag_off(0, g, dst & 15) : OOB);
      f32x4 acc[5];
      StatLd st;
      if (w < 4) {
        v4u xf[20];
        __builtin_amdgcn_s_sleep(XDELAY);
        kx_issue(xf, ws, xo, v16);
        stat_issue(st, ws, ly * 3 + 0, r * 4 + w, lane);
        BAR();
        stamp(k, sb + 8);
        acc_zero(acc);
        KMAIN(P_D, 2, true)
        wait_tie_stat<0>(st);
        if (__builtin_expect(__any(umax(umax(st.s0.x, st.s0.y), umax(st.s1.x, st.s1.y)) == POISON), 0)) {
#pragma unroll 1
          for (int spins = 0; spins < (1 << 18); ++spins) {
            stat_issue(st, ws, ly * 3 + 0, r * 4 + w, lane); wait_tie_stat<0>(st);
            if (!__any(umax(umax(st.s0.x, st.s0.y), umax(st.s1.x, st.s1.y)) == POISON)) break;
          }
        }
      } else {
        WAIT_VM(0);
        BAR();
        hstream(k, ws, wt + WT_2, P_E * 5, 55, v16);
        undef5(acc); asm volatile("" : "=v"(st.s0), "=v"(st.s1));
      }
      BAR_L();
      stamp(k, sb + 9);
      if (w < 4) {
        float mean, var; stat_finish(st, mean, var);
        const float rstd = __builtin_amdgcn_rsqf(var + 1e-5f);
        const float sc2 = rstd * __builtin_amdgcn_rsqf(var * rstd * rstd + 1e-5f);
#pragma unroll
        for (int j = 0; j < 5; ++j)
          st64(canon2(ln_gelu4(acc[j], *(LAS f32x4*)(k.L + L_CSL + (16 + j) * 64 + g * 16), -mean, sc2)), dof, ws, act + A_HS + blk_off(5 * c + j));
        if (r == 0 || r == 31) {
          const bool z = shift > 0 ? tok < shift : tok >= S_ + shift;
          const unsigned zof = z ? (unsigned)(tok >> 6) * 81920u + (unsigned)((tok >> 4) & 3) * 1024u + frag_off(0, g, tok & 15) : OOB;
#pragma unroll
          for (int j = 0; j < 5; ++j) st64(v2u{0u, 0u}, zof, ws, act + A_HS + blk_off(5 * c + j));
        }
#pragma unroll
        for (int j = 0; j < 5; ++j) x1r[j] = rnd4((x2r[j] - mean) * rstd);
      } else {
        hstream(k, ws, wt + WT_2 + 55 * 1024, 140, 15, v16);
        hstream(k, ws, wt + WT_2 + 70 * 1024, 0, 30, v16);
        undef5(x1r);
      }
      stamp(k, sb + 10);
    }
    if (STOP_PHASE == layer * 7 + 2) return;
#define F_WLOAD(i) { ld128<0>(wf3[(i) % 4][0], v16, ws, wa3 + (i) * 12288u); ld128<1024>(wf3[(i) % 4][1], v16, ws, wa3 + (i) * 12288u); \
                     ld128<2048>(wf3[(i) % 4][2], v16, ws, wa3 + (i) * 12288u); }
    {
      PHASE_VARS()
      const int lane = opq(lane0);
      const unsigned v16 = pinv(lane * 16), xo = kx_off(k, act + A_HS);
      const Ring rg = mk_ring(lane);
      f32x4 acc[5];
      if (w < 4) {
        v4u xf[20];
        __builtin_amdgcn_s_sleep(XDELAY);
        kx_issue(xf, ws, xo, v16);
        BAR();
        stamp(k, sb + 11);
        acc_zero(acc);
        KMAIN(P_E, 0, true)
      } else {
        WAIT_VM(0);
        BAR();
        hstream(k, ws, wt + WT_4, P_G * 5, 45, v16);
        undef5(acc);
      }
      stamp(k, sb + 12);
      if (w < 4) {
        v2u pk[5];
#pragma unroll
        for (int j = 0; j < 5; ++j) { pk[j] = pk4(x1r[j] + rnd4(acc[j])); x1r[j] = unpk(pk[j]); }
        kstore(k, ws, act + A_X1, pk, lane);
        kstats(k, ws, ly * 3 + 1, x1r, lane);
        WAIT_VM(0);
      } else {
        undef5(x1r);
      }
      BAR_L();
      post_flag(k, fl_group(ph0 + PH_X1, r, c), tag_of(k, ph0 + PH_X1));
      stamp(k, sb + 13);
    }
    if (STOP_PHASE == layer * 7 + 3) return;
    {
      PHASE_VARS()
      const int lane = opq(lane0), t = lane & 15, g = lane >> 4, cp = w & 3, kh = w >> 2;
      const unsigned v16 = pinv(lane * 16);
      const unsigned wa3 = pin(wt + WT_3 + (unsigned)(kh * 10) * 12288u + cp * 3072u);
      const int ka = (w & 1) + ((w >> 1) & 1) * 10 + (w >> 2) * 5;
      const unsigned xa = pin((unsigned)ka * 4096u), xb2 = pin((unsigned)(4 + ((w >> 1) & 1) * 10 + (w >> 2) * 5) * 4096u + (w & 1) * 2048u);
      const unsigned xsrc = pin(act + A_X1 + (unsigned)r * 81920u);
      v4u wf3[4][3];
#pragma unroll
      for (int i = 0; i < 4; ++i) asm volatile("" : "=v"(wf3[i][0]), "=v"(wf3[i][1]), "=v"(wf3[i][2]));
#define F_WINDOW() { ld128i<0>(wf3[0][0], v16, ws, wa3); ld128i<1024>(wf3[0][1], v16, ws, wa3); ld128i<2048>(wf3[0][2], v16, ws, wa3); }
      if (w != 7) F_WINDOW()
      poll_flags(k, fl_group(ph0 + PH_X1, r, lane & 7), tag_of(k, ph0 + PH_X1));
      BAR();
      FST(41)
      gln<4>(L_XF + xa, v16, ws, xsrc + xa); gln<4>(L_XF + xa + 8192u, v16, ws, xsrc + xa + 8192u); gln<2>(L_XF + xb2, v16, ws, xsrc + xb2);
      if (w == 7) F_WINDOW()
#undef F_WINDOW
      f32x4 acc[3][4];
      acc_zero(acc[0]); acc_zero(acc[1]); acc_zero(acc[2]);
      if (w < 4) WAIT_VM(6); else if (w < 7) WAIT_VM(10);
      BAR();
      StatLd st;
      F_WLOAD(1) F_WLOAD(2) F_WLOAD(3)
      stamp(k, sb + 14);
      FST(29)
      v4u xf[2][4], xn[4];
      LAS unsigned char* xb = lds_at(L_XF + lane * 16 + kh * 40960);
#define F_READ(i) { _Pragma("unroll") for (int tb = 0; tb < 4; ++tb) xf[(i) & 1][tb] = *(LAS v4u*)(xb + ((i) * 4 + tb) * 1024); }
#define F_IT(i, N)                                                                                    \
  {                                                                                                   \
    wait_tie3<N>(wf3[(i) % 4][0], wf3[(i) % 4][1], wf3[(i) % 4][2]);                                  \
    if ((i) == 1) BAR();                    \
    _Pragma("unroll") for (int tb = 0; tb < 4; ++tb) {                                                \
      mfma_ip(acc[0][tb], wf3[(i) % 4][0], xf[(i) & 1][tb]);                                          \
      mfma_ip(acc[1][tb], wf3[(i) % 4][1], xf[(i) & 1][tb]);                                          \
      mfma_ip(acc[2][tb], wf3[(i) % 4][2], xf[(i) & 1][tb]);                                          \
      if ((i) + 1 < 10) xn[tb] = *(LAS v4u*)(xb + (((i) + 1) * 4 + tb) * 1024);                       \
    }                                                                                                 \
    if ((i) + 1 < 10) { _Pragma("unroll") for (int tb = 0; tb < 4; ++tb) xf[((i) + 1) & 1][tb] = xn[tb]; } \
    if ((i) + 4 < 10) F_WLOAD((i) + 4)                                                                \
    if ((i) == 1) stat_issue(st, ws, ly * 3 + 1, r * 4 + (w & 3), lane);                              \
  }
      F_READ(0)
#define F_W7() if (w == 7) WAIT_VM(9);
      F_W7() F_IT(0, 63) F_IT(1, 9) FST(30) F_IT(2, 11) FST(31) F_IT(3, 11) F_IT(4, 11) FST(32) F_IT(5, 11) F_IT(6, 9) F_IT(7, 6) F_IT(8, 3) F_IT(9, 0)
#undef F_IT
#undef F_W7
#undef F_READ
      acc_fence(acc[0]); acc_fence(acc[1]); acc_fence(acc[2]);
      {
        wait_tie_stat<0>(st);
        float mean, var; stat_finish(st, mean, var);
        if (lane < 16) *(LAS f32x2*)(k.L + L_STATS + ((w & 3) * 16 + lane) * 8) = f32x2{mean, __builtin_amdgcn_rsqf(var + 1e-5f)};
      }
      BAR_L();
      stamp(k, sb + 15);
      f32x4 fin[3][2];
      {
        LAS f32x4* sc = (LAS f32x4*)(k.L + L_XF + v16);
        if (kh == 0) {
#pragma unroll
          for (int bl = 0; bl < 3; ++bl) { sc[((cp * 2 + 1) * 6 + bl * 2) * 64] = acc[bl][2]; sc[((cp * 2 + 1) * 6 + bl * 2 + 1) * 64] = acc[bl][3];
                                           fin[bl][0] = acc[bl][0]; fin[bl][1] = acc[bl][1]; }
        } else {
#pragma unroll
          for (int bl = 0; bl < 3; ++bl) { sc[((cp * 2) * 6 + bl * 2) * 64] = acc[bl][0]; sc[((cp * 2) * 6 + bl * 2 + 1) * 64] = acc[bl][1];
                                           fin[bl][0] = acc[bl][2]; fin[bl][1] = acc[bl][3]; }
        }
        BAR_L();
#pragma unroll
        for (int bl = 0; bl < 3; ++bl) { fin[bl][0] += sc[((cp * 2 + kh) * 6 + bl * 2) * 64]; fin[bl][1] += sc[((cp * 2 + kh) * 6 + bl * 2 + 1) * 64]; }
      }
      FST(33)
      const unsigned lo = frag_off(0, g, t);
#pragma unroll
      for (int u = 0; u < 2; ++u) {
        const int tb = 2 * kh + u;
        const f32x2 ms = *(LAS f32x2*)(k.L + L_STATS + (tb * 16 + t) * 8);
#pragma unroll
        for (int bl = 0; bl < 3; ++bl) {
          const int B = 12 * c + 3 * cp + bl;
          st64(canon2(ln_gelu4(fin[bl][u], *(LAS f32x4*)(k.L + L_CSL + (21 + 3 * cp + bl) * 64 + g * 16), -ms.x, ms.y)), lo, ws,
               act + A_H2 + (unsigned)r * 196608u + tb * 1024u + blk_off(B));
        }
      }
      FST(34)
      BAR_L();
      stamp(k, sb + 16);
      hstream(k, ws, wt + WT_4 + 45 * 1024, 75, 40, v16);
    }
#undef F_WLOAD
    if (STOP_PHASE == layer * 7 + 4) return;
    {
      PHASE_VARS()
      const int lane = opq(lane0), t = lane & 15, g = lane >> 4;
      const unsigned v16 = pinv(lane * 16);
      const unsigned xo = pin(act + A_H2 + (unsigned)r * 196608u + (unsigned)(w & 3) * 1024u);
      const Ring rg = mk_ring(lane);
      f32x4 acc[5];
      if (w < 4) {
        __builtin_amdgcn_s_sleep(GDELAY);
        stamp(k, sb + 17);
        v4u xf[16];
#define GX(i) ld128<0>(xf[(i) & 15], v16, ws, xo + (i) * 4096u);
        GX(0) GX(1) GX(2) GX(3) GX(4) GX(5) GX(6) GX(7) GX(8) GX(9) GX(10) GX(11) GX(12) GX(13) GX(14) GX(15)
        acc_zero(acc);
        v4u wf[3][5];
        ring_read<P_G>(rg, wf[0]); ring_read<P_G + 1>(rg, wf[1]);
#define G_STEP(i)                                                                                     \
  {                                                                                                   \
    if ((i) == 7 || (i) == 15 || (i) == 28 || (i) == 36) BAR_L();                                     \
    if ((i) % 4 == 0) {          \
      wait_tie4<((i) + 15 < 47 ? 12 : 44 - (i))>(xf[(i) & 15], xf[((i) + 1) & 15], xf[((i) + 2) & 15], xf[((i) + 3) & 15]); \
      xcheck4(xf[(i) & 15], xf[((i) + 1) & 15], xf[((i) + 2) & 15], xf[((i) + 3) & 15], v16, ws, xo + (i) * 4096u); \
    }                                                                                                 \
    kmma<P_G + (i) + 2, ((i) + 2 < 48)>(rg, acc, wf[(i) % 3], xf[(i) & 15], wf[((i) + 2) % 3]);       \
    if ((i) + 16 < 48) GX((i) + 16 < 48 ? (i) + 16 : 0)                                               \
  }
        G_STEP(0) G_STEP(1) FST(35) G_STEP(2) G_STEP(3) G_STEP(4) G_STEP(5) G_STEP(6) G_STEP(7) FST(36)
        G_STEP(8) G_STEP(9) G_STEP(10) G_STEP(11) G_STEP(12) G_STEP(13) G_STEP(14) G_STEP(15)
        G_STEP(16) G_STEP(17) G_STEP(18) G_STEP(19) G_STEP(20) G_STEP(21) G_STEP(22) G_STEP(23) FST(37)
        G_STEP(24) G_STEP(25) G_STEP(26) G_STEP(27) G_STEP(28) G_STEP(29) G_STEP(30) G_STEP(31)
        G_STEP(32) G_STEP(33) G_STEP(34) G_STEP(35) G_STEP(36) G_STEP(37) G_STEP(38) G_STEP(39) FST(38)
        G_STEP(40) G_STEP(41) G_STEP(42) G_STEP(43) G_STEP(44) G_STEP(45) G_STEP(46) G_STEP(47)
        acc_fence(acc);
#undef G_STEP
#undef GX
      } else {
        hstream(k, ws, wt + WT_4 + 85 * 1024, 115, 40, v16);
        hstream(k, ws, wt + WT_4 + 125 * 1024, 0, 30, v16);
        WAIT_VM(20);
        BAR();
        hstream(k, ws, wt + WT_4 + 155 * 1024, P_G * 5, 40, v16);
        WAIT_VM(12);
        BAR();
        hstream(k, ws, wt + WT_4 + 195 * 1024, (P_G + 8) * 5, 45, v16);
        WAIT_VM(16);
        BAR();
        WAIT_VM(0);
        BAR();
        undef5(acc);
      }
      stamp(k, sb + 18);
      if (w < 4) {
        v2u pk[5];
#pragma unroll
        for (int j = 0; j < 5; ++j) { pk[j] = pk4(x2r[j] + rnd4(x1r[j] + rnd4(acc[j]))); xres[j] = unpk(pk[j]); }
        if (ly == 0) {
          kstore(k, ws, WS_ACT + A_XO, pk, lane);
          kstats(k, ws, 2, xres, lane);
          WAIT_VM(0);
        } else {
          unsigned char* po = opqp(out) + (size_t)(r * 64 + w * 16 + t) * 1280 + (80 * c + 4 * g) * 2;
#pragma unroll
          for (int j = 0; j < 5; ++j) *(GAS v2u*)(po + 32 * j) = pk[j];
        }
      } else {
        undef5(xres);
      }
      if (ly == 0) {
        BAR_L();
        post_flag(k, fl_group(PH_XO, r, c), tag_of(k, PH_XO));
      }
      stamp(k, sb + 19);
    }
    if (STOP_PHASE == layer * 7 + 5) return;
  }
  stampc(k, 127); stamp(k, 125); stamp_dump(k);
  if (k.b == 0 && tid == 0) __hip_atomic_store((GAS u64*)(wsp + WS_EPOCH), k.tag + 64, __ATOMIC_RELAXED, __HIP_MEMORY_SCOPE_AGENT);
}

extern "C" void kernel_launch(void* const* d_in, const int* in_sizes, int n_in, void* d_out, int out_size, void* d_ws, size_t ws_size,
                              hipStream_t stream) {
  (void)in_sizes; (void)n_in; (void)out_size; (void)ws_size;
  (void)hipFuncSetAttribute((const void*)hedgehog_smlp_kernel, hipFuncAttributeMaxDynamicSharedMemorySize, LDS_BYTES);
  hedgehog_smlp_kernel<<<256, 512, LDS_BYTES, stream>>>(
      (const unsigned char*)d_in[0], (const unsigned char*)d_in[1], (const unsigned char*)d_in[2], (const unsigned char*)d_in[3],
      (const unsigned char*)d_in[5], (const unsigned char*)d_in[6], (const unsigned char*)d_in[7], (const unsigned char*)d_in[8],
      (unsigned char*)d_out, (unsigned char*)d_ws);
}
```

```cpp
#include <hip/hip_runtime.h>
#include <hip/hip_bf16.h>
#include <stdint.h>

#define DI __device__ __forceinline__
#define LAS __attribute__((address_space(3)))
#define GAS __attribute__((address_space(1)))
typedef unsigned v4u __attribute__((ext_vector_type(4)));
typedef unsigned v2u __attribute__((ext_vector_type(2)));
typedef float f32x4 __attribute__((ext_vector_type(4)));
typedef float f32x2 __attribute__((ext_vector_type(2)));
typedef short bf16x8 __attribute__((ext_vector_type(8)));
typedef short s16x4 __attribute__((ext_vector_type(4)));
typedef __bf16 bf2_t __attribute__((ext_vector_type(2)));
typedef __amdgpu_buffer_rsrc_t srd_t;
typedef unsigned long long u64;

#define STOP_PHASE 1000
#define STAMPS 0
#define FINE 0
#define LATEW(w) ((w) == 7)
#define XDELAY 14
#define GDELAY 28
#define X0N 8
#define FST(id) { if (FINE) stamp(k, sb + (id)); }

constexpr int S_ = 2048, D_ = 640, F_ = 1536;
constexpr unsigned WS_EPOCH = 0, WS_TMO = 64;
constexpr unsigned WS_FLAGS = 4096;
constexpr unsigned WS_PCS = 512u << 10;
constexpr unsigned PCS_C = 20 * 33 * 64;
constexpr unsigned WS_STAMP = 131072;
constexpr unsigned WS_RED = 1u << 20;
constexpr unsigned WS_STAT = WS_RED + (1u << 19);
constexpr unsigned STAT_SZ = 128 * 1024;
constexpr unsigned WS_PART = 4u << 20;
constexpr unsigned PART_L = 256u * 16384u;
constexpr unsigned ACT20 = 128u * 20u * 1024u, ACT48 = 128u * 48u * 1024u;
constexpr unsigned WS_ACT = 16u << 20;
constexpr unsigned ACT_L = 5u * ACT20 + ACT48;
constexpr unsigned A_O = 0, A_X2 = ACT20, A_HS = 2 * ACT20, A_X1 = 3 * ACT20, A_XO = 4 * ACT20, A_H2 = 5 * ACT20;
constexpr unsigned WS_WT = 64u << 20;
constexpr unsigned WT_A = 0, WT_O = 327680, WT_1 = WT_O + 102400, WT_2 = WT_1 + 102400, WT_4 = WT_2 + 102400, WT_3 = WT_4 + 245760,
                   WT_C = WT_3 + 245760;
constexpr unsigned WS_END = WS_WT + 8 * WT_C;
enum { PH_KV1 = 0, PH_KV2, PH_O, PH_X2, PH_H, PH_X1, PH_H2, PH_XO, PH_PER_LAYER, PH_T = 16, PH_TG = 17 };
constexpr unsigned SLOT = 5120, NSLOT = 31, RINGP = 155;
constexpr unsigned L_MISC = SLOT * NSLOT;
constexpr unsigned L_STATS = L_MISC;
constexpr unsigned L_CSL = L_MISC + 512;
constexpr unsigned LDS_BYTES = 163840;
constexpr unsigned L_X = 0, L_XF = 76800;
constexpr unsigned L_IMG = 0, IMG_H = 10240;
constexpr unsigned L_S0 = 81920, L_SJ = 97280, L_SPH = 146432;
constexpr int P_C = 8, P_D = 28, P_E = 17, P_G = 6;
constexpr unsigned OOB = 0x80000000u;

#define WAIT_VM(n) asm volatile("s_waitcnt vmcnt(" #n ")" ::: "memory")
#define BAR() asm volatile("s_barrier" ::: "memory")
#define BAR_L() asm volatile("s_waitcnt lgkmcnt(0)\n\ts_barrier" ::: "memory")
#define BAR_VL0() asm volatile("s_waitcnt vmcnt(0) lgkmcnt(0)\n\ts_barrier" ::: "memory")
#define SB() __builtin_amdgcn_sched_barrier(0)
template <int N> DI void bar_vl() { asm volatile("s_waitcnt vmcnt(%0) lgkmcnt(0)\n\ts_barrier" ::"i"(N) : "memory"); }

DI void glds(unsigned lds, unsigned voff, srd_t srd, unsigned soff) {
  asm volatile("s_mov_b32 m0, %0\n\ts_nop 0\n\tbuffer_load_dwordx4 %1, %2, %3 offen lds" ::"s"(lds), "v"(voff), "s"(srd), "s"(soff) : "memory");
}
template <int N> DI void gln(unsigned lds, unsigned voff, srd_t srd, unsigned soff);
template <> DI void gln<1>(unsigned lds, unsigned voff, srd_t srd, unsigned soff) {
  asm volatile("s_mov_b32 m0, %0\n\ts_nop 0\n\tbuffer_load_dwordx4 %1, %2, %3 offen sc1 lds" ::"s"(lds), "v"(voff), "s"(srd), "s"(soff) : "memory");
}
template <> DI void gln<2>(unsigned lds, unsigned voff, srd_t srd, unsigned soff) {
  asm volatile("s_mov_b32 m0, %0\n\ts_nop 0\n\tbuffer_load_dwordx4 %1, %2, %3 offen sc1 lds\n\t"
               "buffer_load_dwordx4 %1, %2, %3 offen offset:1024 sc1 lds" ::"s"(lds), "v"(voff), "s"(srd), "s"(soff) : "memory");
}
template <> DI void gln<3>(unsigned lds, unsigned voff, srd_t srd, unsigned soff) {
  asm volatile("s_mov_b32 m0, %0\n\ts_nop 0\n\tbuffer_load_dwordx4 %1, %2, %3 offen sc1 lds\n\t"
               "buffer_load_dwordx4 %1, %2, %3 offen offset:1024 sc1 lds\n\tbuffer_load_dwordx4 %1, %2, %3 offen offset:2048 sc1 lds"
               ::"s"(lds), "v"(voff), "s"(srd), "s"(soff) : "memory");
}
template <> DI void gln<4>(unsigned lds, unsigned voff, srd_t srd, unsigned soff) {
  asm volatile("s_mov_b32 m0, %0\n\ts_nop 0\n\tbuffer_load_dwordx4 %1, %2, %3 offen sc1 lds\n\t"
               "buffer_load_dwordx4 %1, %2, %3 offen offset:1024 sc1 lds\n\tbuffer_load_dwordx4 %1, %2, %3 offen offset:2048 sc1 lds\n\t"
               "buffer_load_dwordx4 %1, %2, %3 offen offset:3072 sc1 lds" ::"s"(lds), "v"(voff), "s"(srd), "s"(soff) : "memory");
}
template <> DI void gln<6>(unsigned lds, unsigned voff, srd_t srd, unsigned soff);
template <> DI void gln<5>(unsigned lds, unsigned voff, srd_t srd, unsigned soff) { gln<4>(lds, voff, srd, soff); gln<1>(lds + 4096, voff, srd, soff + 4096); }
template <> DI void gln<6>(unsigned lds, unsigned voff, srd_t srd, unsigned soff) { gln<4>(lds, voff, srd, soff); gln<2>(lds + 4096, voff, srd, soff + 4096); }
template <int P> DI void gl1(unsigned lds, unsigned voff, srd_t srd, unsigned soff) {
  asm volatile("s_mov_b32 m0, %0\n\ts_nop 0\n\tbuffer_load_dwordx4 %1, %2, %3 offen offset:%4 sc1 lds"
               ::"s"(lds + (P >> 2) * 4096u), "v"(voff), "s"(srd), "s"(soff + (P >> 2) * 4096u), "i"((P & 3) * 1024) : "memory");
}
template <int OFF> DI void ld128(v4u& d, unsigned voff, srd_t srd, unsigned soff) {
  asm volatile("buffer_load_dwordx4 %0, %1, %2, %3 offen offset:%4 sc1" : "=v"(d) : "v"(voff), "s"(srd), "s"(soff), "i"(OFF) : "memory");
}
template <int OFF> DI void ld64(v2u& d, unsigned voff, srd_t srd, unsigned soff) {
  asm volatile("buffer_load_dwordx2 %0, %1, %2, %3 offen offset:%4 sc1" : "=v"(d) : "v"(voff), "s"(srd), "s"(soff), "i"(OFF) : "memory");
}
template <int N> DI void wait_tie(v4u& a) { asm volatile("s_waitcnt vmcnt(%1)" : "+v"(a) : "i"(N)); }
template <int N> DI void wait_tie2(v4u& a, v4u& b) { asm volatile("s_waitcnt vmcnt(%2)" : "+v"(a), "+v"(b) : "i"(N)); }
template <int N> DI void wait_tie3(v4u& a, v4u& b, v4u& c) { asm volatile("s_waitcnt vmcnt(%3)" : "+v"(a), "+v"(b), "+v"(c) : "i"(N)); }
DI void tie2(v4u& a, v4u& b) { asm volatile("" : "+v"(a), "+v"(b)); }
DI void tie3(v4u& a, v4u& b, v4u& c) { asm volatile("" : "+v"(a), "+v"(b), "+v"(c)); }
DI void tie1(v4u& a) { asm volatile("" : "+v"(a)); }
DI void st128(v4u v, unsigned voff, srd_t srd, unsigned soff) { __builtin_amdgcn_raw_buffer_store_b128(v, srd, voff, soff, 16); }
DI void st64(v2u v, unsigned voff, srd_t srd, unsigned soff) { __builtin_amdgcn_raw_buffer_store_b64(v, srd, voff, soff, 16); }

DI float asf(unsigned u) { return __builtin_bit_cast(float, u); }
DI unsigned asu(float f) { return __builtin_bit_cast(unsigned, f); }
DI srd_t mk_srd(const void* p, unsigned bytes) { return __builtin_amdgcn_make_buffer_rsrc((void*)p, 0, bytes, 0x00020000); }
DI f32x4 mfma(v4u a, v4u b, f32x4 c) {
  return __builtin_amdgcn_mfma_f32_16x16x32_bf16(__builtin_bit_cast(bf16x8, a), __builtin_bit_cast(bf16x8, b), c, 0, 0, 0);
}
DI void mfma_ip(f32x4& c, const v4u& a, const v4u& b) { asm volatile("v_mfma_f32_16x16x32_bf16 %0, %1, %2, %0" : "+v"(c) : "v"(a), "v"(b)); }
template <int N> DI void acc_fence(f32x4 (&a)[N]) {
  asm volatile("s_nop 15\n\ts_nop 7" ::: "memory");
#pragma unroll
  for (int i = 0; i < N; ++i) asm volatile("" : "+v"(a[i]));
}
template <int N> DI void acc_zero(f32x4 (&a)[N]) {
#pragma unroll
  for (int i = 0; i < N; ++i) { a[i] = f32x4{0.f, 0.f, 0.f, 0.f}; asm volatile("" : "+v"(a[i])); }
  asm volatile("s_nop 1");
}
DI unsigned pk2(float a, float b) { f32x2 v = {a, b}; return __builtin_bit_cast(unsigned, __builtin_convertvector(v, bf2_t)); }
DI unsigned pk2v(f32x2 v) { return __builtin_bit_cast(unsigned, __builtin_convertvector(v, bf2_t)); }
DI float bflo(unsigned u) { return asf(u << 16); }
DI float bfhi(unsigned u) { return asf(u & 0xFFFF0000u); }
DI f32x4 unpk(v2u u) { return f32x4{bflo(u.x), bfhi(u.x), bflo(u.y), bfhi(u.y)}; }
DI v2u pk4(f32x4 v) { return v2u{pk2(v.x, v.y), pk2(v.z, v.w)}; }
DI f32x4 rnd4(f32x4 v) { return unpk(pk4(v)); }
template <int ROW8> DI v4u trfrag(LAS unsigned char* p) {
  s16x4 lo = __builtin_amdgcn_ds_read_tr16_b64_v4i16((LAS s16x4*)p);
  s16x4 hi = __builtin_amdgcn_ds_read_tr16_b64_v4i16((LAS s16x4*)(p + ROW8));
  return __builtin_bit_cast(v4u, __builtin_shufflevector(lo, hi, 0, 1, 2, 3, 4, 5, 6, 7));
}
DI int tr_row(int lane) { const int g = lane >> 4; return 16 * (g >> 1) + 4 * (g & 1) + ((lane >> 2) & 3); }
DI void xr_sum2(float& a, float& b) {
  auto s = __builtin_amdgcn_permlane16_swap(asu(a), asu(b), false, false);
  const float m = asf(s[0]) + asf(s[1]);
  auto t = __builtin_amdgcn_permlane32_swap(asu(m), asu(m), false, false);
  const float n = asf(t[0]) + asf(t[1]);
  auto u = __builtin_amdgcn_permlane16_swap(asu(n), asu(n), false, false);
  a = asf(u[0]); b = asf(u[1]);
}
DI float fmax_raw(float a, float b) { float r; asm("v_max_f32 %0, %1, %2" : "=v"(r) : "v"(a), "v"(b)); return r; }
DI float fmax3_raw(float a, float b, float c) { float r; asm("v_max3_f32 %0, %1, %2, %3" : "=v"(r) : "v"(a), "v"(b), "v"(c)); return r; }
DI float fmin3_raw(float a, float b, float c) { float r; asm("v_min3_f32 %0, %1, %2, %3" : "=v"(r) : "v"(a), "v"(b), "v"(c)); return r; }
DI void xr_max2(float& a, float& b) {
  auto s = __builtin_amdgcn_permlane16_swap(asu(a), asu(b), false, false);
  const float m = __builtin_fmaxf(asf(s[0]), asf(s[1]));
  auto t = __builtin_amdgcn_permlane32_swap(asu(m), asu(m), false, false);
  const float n = __builtin_fmaxf(asf(t[0]), asf(t[1]));
  auto u = __builtin_amdgcn_permlane16_swap(asu(n), asu(n), false, false);
  a = asf(u[0]); b = asf(u[1]);
}
template <bool MAX> DI void xr4(float (&a)[4], float (&b)[4]) {
  float m[4], n[4];
#pragma unroll
  for (int i = 0; i < 4; ++i) { auto s = __builtin_amdgcn_permlane16_swap(asu(a[i]), asu(b[i]), false, false);
                                m[i] = MAX ? fmax_raw(asf(s[0]), asf(s[1])) : asf(s[0]) + asf(s[1]); }
#pragma unroll
  for (int i = 0; i < 4; ++i) { auto t = __builtin_amdgcn_permlane32_swap(asu(m[i]), asu(m[i]), false, false);
                                n[i] = MAX ? fmax_raw(asf(t[0]), asf(t[1])) : asf(t[0]) + asf(t[1]); }
#pragma unroll
  for (int i = 0; i < 4; ++i) { auto u = __builtin_amdgcn_permlane16_swap(asu(n[i]), asu(n[i]), false, false); a[i] = asf(u[0]); b[i] = asf(u[1]); }
}
DI f32x2 gelu2(f32x2 x) {
  constexpr float C0 = -2.0f * 0.7978845608028654f * 1.4426950408889634f, C1 = C0 * 0.044715f;
  const f32x2 a = x * ((x * x) * C1 + C0);
  const f32x2 d = f32x2{__builtin_amdgcn_exp2f(a.x), __builtin_amdgcn_exp2f(a.y)} + 1.0f;
  return x * f32x2{__builtin_amdgcn_rcpf(d.x), __builtin_amdgcn_rcpf(d.y)};
}
DI v2u ln_gelu4(f32x4 acc, f32x4 cs, float nmean, float scale) {
  const f32x2 a = (f32x2{cs.x, cs.y} * nmean + f32x2{acc.x, acc.y}) * scale, b = (f32x2{cs.z, cs.w} * nmean + f32x2{acc.z, acc.w}) * scale;
  return v2u{pk2v(gelu2(a)), pk2v(gelu2(b))};
}
DI void undef5(f32x4 (&a)[5]) { asm volatile("" : "=v"(a[0]), "=v"(a[1]), "=v"(a[2]), "=v"(a[3]), "=v"(a[4])); }
DI void undef5u(v4u (&a)[5]) { asm volatile("" : "=v"(a[0]), "=v"(a[1]), "=v"(a[2]), "=v"(a[3]), "=v"(a[4])); }
DI int opq(int x) { asm volatile("" : "+v"(x)); return x; }
DI int opqs(int x) { asm volatile("" : "+s"(x)); return x; }
template <class T> DI T* opqp(T* p) { asm volatile("" : "+s"(p)); return p; }

struct Ctx {
  unsigned char* ws;
  LAS unsigned char* L;
  int w, r, c, b;
  u64 tag;
  bool dead;
  DI void fresh() { w = opqs(w); r = opqs(r); c = opqs(c); ws = opqp(ws); }
};

DI void stamp(const Ctx&, int) {}
DI void stampc(const Ctx&, int) {}
DI void stamp_dump(const Ctx&) {}
DI void stampw(const Ctx&, int) {}

DI void poll_flags(Ctx& k, unsigned flag_byte_off, u64 want) {
  if (k.w == 7 && !k.dead) {
    GAS u64* p = (GAS u64*)(k.ws + WS_FLAGS + flag_byte_off);
    unsigned spins = 0;
    for (;;) {
      const u64 v = __hip_atomic_load(p, __ATOMIC_RELAXED, __HIP_MEMORY_SCOPE_AGENT);
      if (__all(v == want)) break;
      if (++spins > (1u << 20)) { k.dead = true; __hip_atomic_store((GAS unsigned*)(k.ws + WS_TMO), 1u, __ATOMIC_RELAXED, __HIP_MEMORY_SCOPE_AGENT); break; }
    }
  }
}
DI void poll_flags_any(Ctx& k, unsigned flag_byte_off, u64 want) {
  if (!k.dead) {
    GAS u64* p = (GAS u64*)(k.ws + WS_FLAGS + flag_byte_off);
    unsigned spins = 0;
    for (;;) {
      const u64 v = __hip_atomic_load(p, __ATOMIC_RELAXED, __HIP_MEMORY_SCOPE_AGENT);
      if (__all(v == want)) break;
      if (++spins > (1u << 20)) { k.dead = true; __hip_atomic_store((GAS unsigned*)(k.ws + WS_TMO), 1u, __ATOMIC_RELAXED, __HIP_MEMORY_SCOPE_AGENT); break; }
    }
  }
}
DI v4u granule(f32x4 v, u64 tg) { return v4u{pk2(v.x, v.y), (unsigned)tg, pk2(v.z, v.w), (unsigned)(tg >> 32)}; }
DI bool granule_ok(v4u g, u64 tg) { return g.y == (unsigned)tg && g.w == (unsigned)(tg >> 32); }
DI bool spin_more(Ctx& k, unsigned& spins) {
  if (k.dead) return false;
  __builtin_amdgcn_s_sleep(2);
  if (++spins > (1u << 19)) { k.dead = true; __hip_atomic_store((GAS unsigned*)(k.ws + WS_TMO), 1u, __ATOMIC_RELAXED, __HIP_MEMORY_SCOPE_AGENT); return false; }
  return true;
}
DI void post_flag(const Ctx& k, unsigned flag_byte_off, u64 v) {
  if (threadIdx.x == 7 * 64) __hip_atomic_store((GAS u64*)(k.ws + WS_FLAGS + flag_byte_off), v, __ATOMIC_RELAXED, __HIP_MEMORY_SCOPE_AGENT);
}
DI u64 tag_of(const Ctx& k, int ph) { return k.tag + (u64)(ph + 1); }
DI unsigned fl_group(int ph, int r, int c) { return (unsigned)((ph * 256 + r * 8 + c) * 8); }
DI unsigned fl_col(int ph, int r, int c) { return (unsigned)((ph * 256 + c * 32 + r) * 8); }

constexpr unsigned POISON = 0xFFFFFFFFu;
DI unsigned canon(unsigned u) { return u < POISON - 1u ? u : POISON - 1u; }
DI v2u canon2(v2u a) { return v2u{canon(a.x), canon(a.y)}; }
DI unsigned umax(unsigned a, unsigned b) { return a > b ? a : b; }
DI unsigned umax4(v4u x) { return umax(umax(x.x, x.y), umax(x.z, x.w)); }
DI bool poisoned4(v4u a, v4u b, v4u c, v4u d) { return __any(umax(umax(umax4(a), umax4(b)), umax(umax4(c), umax4(d))) == POISON); }
template <int N> DI void wait_tie4(v4u& a, v4u& b, v4u& c, v4u& d) { asm volatile("s_waitcnt vmcnt(%4)" : "+v"(a), "+v"(b), "+v"(c), "+v"(d) : "i"(N)); }
DI void xcheck4(v4u& a, v4u& b, v4u& c, v4u& d, unsigned vo, srd_t ws, unsigned so) {
  if (__builtin_expect(poisoned4(a, b, c, d), 0)) {
#pragma unroll 1
    for (int spins = 0; spins < (1 << 18); ++spins) {
      if (FINE == 7 && (threadIdx.x & 63) == 0) __hip_atomic_fetch_add((LAS unsigned*)(size_t)(L_MISC + 2816 + 150 * 8), 1u, __ATOMIC_RELAXED, __HIP_MEMORY_SCOPE_WORKGROUP);
      __builtin_amdgcn_s_sleep(1);
      ld128<0>(a, vo, ws, so); ld128<0>(b, vo, ws, so + 4096u); ld128<0>(c, vo, ws, so + 8192u); ld128<0>(d, vo, ws, so + 12288u);
      wait_tie4<0>(a, b, c, d);
      if (!poisoned4(a, b, c, d)) break;
    }
  }
}

template <int N> DI void ldscheck(const Ctx& k, unsigned lds, unsigned v16, srd_t ws, unsigned src) {
  LAS unsigned char* p = k.L + lds + v16;
  unsigned m = 0;
#pragma unroll
  for (int i = 0; i < N; ++i) m = umax(m, umax4(*(LAS v4u*)(p + i * 1024)));
  if (__builtin_expect(__any(m == POISON), 0)) {
#pragma unroll 1
    for (int spins = 0; spins < (1 << 18); ++spins) {
      __builtin_amdgcn_s_sleep(1);
      gln<N>(lds, v16, ws, src);
      WAIT_VM(0);
      m = 0;
#pragma unroll
      for (int i = 0; i < N; ++i) m = umax(m, umax4(*(volatile LAS v4u*)(p + i * 1024)));
      if (!__any(m == POISON)) break;
    }
  }
}

template <int NW> DI void wstream(const Ctx& k, srd_t ws, unsigned src, int lp, int np, unsigned v16) {
  if (k.w < NW) {
#pragma unroll 1
    for (int g = k.w * 4; g < np; g += NW * 4) {
      const int s = g + 4 > np ? np - 4 : g;
      gln<4>((unsigned)(lp + s) * 1024u, v16, ws, src + (unsigned)s * 1024u);
    }
  }
}
template <int G> DI void wstream_exact(const Ctx& k, srd_t ws, unsigned src, int lp, int np, unsigned v16) {
  const int g = k.w * G, s = g + G > np ? np - G : g;
  gln<G>((unsigned)(lp + s) * 1024u, v16, ws, src + (unsigned)s * 1024u);
}

struct StatLd { v2u s0, s1; };
DI void stat_issue(StatLd& s, srd_t ws, unsigned statbuf, int tb, int lane) {
  const unsigned vo = (unsigned)((lane >> 4) * 256 + (lane & 15) * 8);
  const unsigned so = WS_STAT + statbuf * STAT_SZ + (unsigned)tb * 1024u;
  ld64<0>(s.s0, vo, ws, so); ld64<128>(s.s1, vo, ws, so);
}
DI void stat_finish(const StatLd& s, float& mean, float& var) {
  float a = asf(s.s0.x) + asf(s.s1.x), q = asf(s.s0.y) + asf(s.s1.y);
  xr_sum2(a, q);
  mean = a * (1.0f / 640.0f);
  var = __builtin_fmaxf(__builtin_fmaf(-mean, mean, q * (1.0f / 640.0f)), 0.0f);
}
template <int N> DI void wait_tie_stat(StatLd& s);
DI bool stat_poisoned(const StatLd& st) { return __any(umax(umax(st.s0.x, st.s0.y), umax(st.s1.x, st.s1.y)) == POISON); }
template <int N> DI void wait_tie_stat(StatLd& s) { asm volatile("s_waitcnt vmcnt(%2)" : "+v"(s.s0), "+v"(s.s1) : "i"(N)); }

DI void statcheck(StatLd& st, srd_t ws, unsigned statbuf, int tb, int lane) {
  if (__builtin_expect(stat_poisoned(st), 0)) {
#pragma unroll 1
    for (int spins = 0; spins < (1 << 18); ++spins) {
      stat_issue(st, ws, statbuf, tb, lane); wait_tie_stat<0>(st);
      if (!stat_poisoned(st)) break;
    }
  }
}

#define MIX(NM, ND) { _Pragma("unroll") for (int m_ = 0; m_ < ND; ++m_) { __builtin_amdgcn_sched_group_barrier(0x008, NM, 0); __builtin_amdgcn_sched_group_barrier(0x100, 1, 0); } }
struct Ring { LAS unsigned char *b0, *b1, *b2; };
DI LAS unsigned char* lds_at(unsigned a) { asm volatile("" : "+v"(a)); return (LAS unsigned char*)(size_t)a; }
DI Ring mk_ring(int lane) { return Ring{lds_at(lane * 16), lds_at(lane * 16 + 12 * SLOT), lds_at(lane * 16 + 24 * SLOT)}; }
template <int N, int BL> DI v4u ring_frag(const Ring& r) {
  constexpr int s = N % 31;
  return *(LAS v4u*)((s < 12 ? r.b0 : s < 24 ? r.b1 : r.b2) + (s % 12) * SLOT + 1024 * BL);
}
template <int N> DI void ring_read(const Ring& r, v4u (&d)[5]) {
  d[0] = ring_frag<N, 0>(r); d[1] = ring_frag<N, 1>(r); d[2] = ring_frag<N, 2>(r); d[3] = ring_frag<N, 3>(r); d[4] = ring_frag<N, 4>(r);
}
template <int N, bool RD> DI void kmma(const Ring& r, f32x4 (&acc)[5], const v4u (&w)[5], const v4u& x, v4u (&nx)[5]) {
  mfma_ip(acc[0], w[0], x); if (RD) nx[0] = ring_frag<N, 0>(r);
  mfma_ip(acc[1], w[1], x); if (RD) nx[1] = ring_frag<N, 1>(r);
  mfma_ip(acc[2], w[2], x); if (RD) nx[2] = ring_frag<N, 2>(r);
  mfma_ip(acc[3], w[3], x); if (RD) nx[3] = ring_frag<N, 3>(r);
  mfma_ip(acc[4], w[4], x); if (RD) nx[4] = ring_frag<N, 4>(r);
}
#define KSTEP(P, NX, NS, i)                                                                   \
  {                                                                                           \
    if (!CHK) wait_tie<kx_wait(i, i, NS)>(xf[i]);                                             \
    else if (i % 4 == 0) {                                                                    \
      wait_tie4<kx_wait(i, i + 3, NS)>(xf[i], xf[(i + 1) % NX], xf[(i + 2) % NX], xf[(i + 3) % NX]); \
      xcheck4(xf[i], xf[(i + 1) % NX], xf[(i + 2) % NX], xf[(i + 3) % NX], v16, ws, xo + i * 4096u); \
    }                                                                                         \
    kmma<P + i + 2, (i + 2 < NX)>(rg, acc, wf[i % 3], xf[i], wf[(i + 2) % 3]);                \
    if (i + X0N < NX) ld128<0>(xf[(i + X0N) % NX], v16, ws, xo + (i + X0N) * 4096u);          \
  }
#define KMAIN(P, NS, CHECK)                                                                   \
  {                                                                                           \
    constexpr bool CHK = CHECK;                                                               \
    v4u wf[3][5];                                                                             \
    ring_read<P>(rg, wf[0]); ring_read<P + 1>(rg, wf[1]);                                     \
    KSTEP(P, 20, NS, 0) KSTEP(P, 20, NS, 1) KSTEP(P, 20, NS, 2) KSTEP(P, 20, NS, 3) KSTEP(P, 20, NS, 4)      \
    KSTEP(P, 20, NS, 5) KSTEP(P, 20, NS, 6) KSTEP(P, 20, NS, 7) KSTEP(P, 20, NS, 8) KSTEP(P, 20, NS, 9)      \
    KSTEP(P, 20, NS, 10) KSTEP(P, 20, NS, 11) KSTEP(P, 20, NS, 12) KSTEP(P, 20, NS, 13) KSTEP(P, 20, NS, 14) \
    KSTEP(P, 20, NS, 15) KSTEP(P, 20, NS, 16) KSTEP(P, 20, NS, 17) KSTEP(P, 20, NS, 18) KSTEP(P, 20, NS, 19) \
    acc_fence(acc);                                                                           \
  }
DI unsigned pin(unsigned x) { asm volatile("" : "+s"(x)); return x; }
DI unsigned pinv(unsigned x) { asm volatile("" : "+v"(x)); return x; }
DI unsigned kx_off(const Ctx& k, unsigned actbuf) { return pin(actbuf + (unsigned)k.r * 81920u + (unsigned)(k.w & 3) * 1024u); }
DI void kx_issue(v4u (&x)[20], srd_t ws, unsigned so, unsigned vo) {
#pragma unroll
  for (int i = 0; i < X0N; ++i) ld128<0>(x[i], vo, ws, so + i * 4096u);
}
constexpr int kx_wait(int i, int j, int ns) { return ((X0N + i - 1 < 19 ? X0N + i - 1 : 19) - j) + (j < X0N ? ns : 0); }
DI void hstream(const Ctx& k, srd_t ws, unsigned src, int lp, int np, unsigned v16) {
  if (k.w >= 4 && k.w < 7) {
#pragma unroll 1
    for (int g = (k.w - 4) * 4; g < np; g += 12) {
      const int s = g + 4 > np ? np - 4 : g;
      gln<4>((unsigned)(lp + s) * 1024u, v16, ws, src + (unsigned)s * 1024u);
    }
  }
}
DI unsigned frag_off(int B, int gq, int t) { return (unsigned)((B >> 1) * 4096 + (2 * (B & 1) + (gq & 1)) * 256 + t * 16 + (gq >> 1) * 8); }
DI unsigned blk_off(int B) { return (unsigned)((B >> 1) * 4096 + (B & 1) * 512); }
DI void kstore(const Ctx& k, srd_t ws, unsigned actbuf, const v2u (&pk)[5], int lane) {
  const unsigned lo = frag_off(0, lane >> 4, lane & 15);
  const unsigned so = actbuf + (unsigned)k.r * 81920u + (unsigned)(k.w & 3) * 1024u;
#pragma unroll
  for (int j = 0; j < 5; ++j) st64(canon2(pk[j]), lo, ws, so + blk_off(5 * k.c + j));
}
DI void kstats(const Ctx& k, srd_t ws, unsigned statbuf, const f32x4 (&v)[5], int lane) {
  f32x2 s = {0.f, 0.f}, q = {0.f, 0.f};
#pragma unroll
  for (int j = 0; j < 5; ++j) {
    const f32x2 a = {v[j].x, v[j].y}, b = {v[j].z, v[j].w};
    s += a + b; q = a * a + q; q = b * b + q;
  }
  float s1 = s.x + s.y, q1 = q.x + q.y;
  xr_sum2(s1, q1);
  const unsigned vo = lane < 16 ? (unsigned)(lane * 8) : OOB;
  st64(v2u{canon(asu(s1)), canon(asu(q1))}, vo, ws, WS_STAT + statbuf * STAT_SZ + (unsigned)((k.r * 4 + (k.w & 3)) * 1024 + k.c * 128));
}
DI void poison_tile(const Ctx& k, srd_t ws, unsigned actbuf, int statbuf, int tb, int lane) {
  const v2u p2 = {POISON, POISON};
  const unsigned lo = frag_off(0, lane >> 4, lane & 15);
#pragma unroll
  for (int j = 0; j < 5; ++j) st64(p2, lo, ws, actbuf + (unsigned)k.r * 81920u + tb * 1024u + blk_off(5 * k.c + j));
  if (statbuf >= 0) st64(p2, lane < 16 ? (unsigned)(lane * 8) : OOB, ws, WS_STAT + (unsigned)statbuf * STAT_SZ + (unsigned)((k.r * 4 + tb) * 1024 + k.c * 128));
}
DI void poison_hs(const Ctx& k, srd_t ws, unsigned act, int tb, int lane) {
  const int r = k.r, c = k.c, t = lane & 15, g = lane >> 4;
  const v2u p2 = {POISON, POISON};
  const int shift = (c >> 1) == 0 ? 1 : (c >> 1) == 1 ? -1 : (c >> 1) == 2 ? 2 : -2;
  const int tok = r * 64 + tb * 16 + t, dst = tok + shift;
  const unsigned dof = (dst >= 0 && dst < S_) ? (unsigned)(dst >> 6) * 81920u + (unsigned)((dst >> 4) & 3) * 1024u + frag_off(0, g, dst & 15) : OOB;
  const bool z = shift > 0 ? tok < shift : tok >= S_ + shift;
  const unsigned zof = z ? (unsigned)(tok >> 6) * 81920u + (unsigned)((tok >> 4) & 3) * 1024u + frag_off(0, g, t) : OOB;
#pragma unroll
  for (int j = 0; j < 5; ++j) {
    st64(p2, dof, ws, act + A_HS + blk_off(5 * c + j));
    if (r == 0 || r == 31) st64(p2, zof, ws, act + A_HS + blk_off(5 * c + j));
  }
}
DI void poison_h2(const Ctx& k, srd_t ws, unsigned act, int tb, int lane) {
#pragma unroll
  for (int u = 0; u < 6; ++u)
    st128(v4u{POISON, POISON, POISON, POISON}, lane * 16, ws, act + A_H2 + (unsigned)k.r * 196608u + (unsigned)(24 * k.c + 6 * tb + u) * 1024u);
}
DI void chunk_sum(v4u f, srd_t ws, int c, int s, int fi, int lane) {
  const v4u ones = {0x3F803F80u, 0x3F803F80u, 0x3F803F80u, 0x3F803F80u};
  const f32x4 cs = mfma(f, ones, f32x4{0.f, 0.f, 0.f, 0.f});
  st128(__builtin_bit_cast(v4u, cs), (lane & 15) == 0 ? (unsigned)(lane >> 4) * 16u : OOB, ws, WS_PCS + (unsigned)c * PCS_C + (unsigned)(s * 33 + fi) * 64u);
}

__global__ void __launch_bounds__(512, 2)
hedgehog_smlp_kernel(const unsigned char* x, const unsigned char* wqkv, const unsigned char* wphi, const unsigned char* wo,
                     const unsigned char* w1, const unsigned char* w2, const unsigned char* w3, const unsigned char* w4,
                     unsigned char* out, unsigned char* wsp) {
  extern __shared__ __attribute__((aligned(16))) unsigned char smem[];
  Ctx k;
  k.ws = wsp;
  k.L = (LAS unsigned char*)smem;
  const int tid = threadIdx.x;
  const int lane0 = tid & 63;
  k.w = __builtin_amdgcn_readfirstlane(tid >> 6);
  k.b = blockIdx.x;
#define MAPMODE 2
  { const int xc = k.b & 7, i = k.b >> 3;
    if (MAPMODE == 0) { k.r = xc * 4 + (i >> 3); k.c = i & 7; }
    else if (MAPMODE == 2) { k.r = (xc >> 1) * 8 + (i >> 2); k.c = (xc & 1) * 4 + (i & 3); }
    else { k.r = (xc >> 2) * 16 + (i >> 1); k.c = (xc & 3) * 2 + (i & 1); } }
  k.dead = false;
  f32x4 xres[5];
  v4u aphi[2];
#define PHASE_VARS()                                                             \
  k.fresh();                                                                     \
  const int w = k.w, r = k.r, c = k.c;                                           \
  const srd_t ws = mk_srd(k.ws, WS_END);                                         \
  const int ly = opqs(layer), ph0 = ly * PH_PER_LAYER, sb = 2 + ly * 64;         \
  const unsigned act = WS_ACT + (unsigned)ly * ACT_L;                            \
  const unsigned wt = WS_WT + (unsigned)c * WT_C;                                \
  (void)w; (void)r; (void)c; (void)ph0; (void)sb; (void)act; (void)wt;
  stamp(k, 0); stampc(k, 126);
#define CODEPF 1
  constexpr int CODE_KB = 38;
  v4u code_sink = {0u, 0u, 0u, 0u};
  if (CODEPF) {
    unsigned long long pc;
    asm volatile("s_getpc_b64 %0" : "=s"(pc));
    const unsigned long long a = pc + (unsigned long long)((((k.b >> 3) * 8 + k.w) % CODE_KB) * 1024 + lane0 * 16);
    asm volatile("global_load_dwordx4 %0, %1, off" : "=v"(code_sink) : "v"(a) : "memory");
  }
  {
    const int w = k.w, r = k.r, c = k.c;
    const srd_t ws = mk_srd(wsp, WS_END);
    const unsigned wt = WS_WT + (unsigned)c * WT_C;
    const int lane = opq(lane0), g = lane >> 4, p = lane & 3, trr = tr_row(lane);
    if (r < 20) {
      const srd_t srd = mk_srd(wqkv + 160 * c + (size_t)r * 122880, 122880 - 160 * c);
#pragma unroll
      for (int u = 0; u < 2; ++u) {
        const unsigned id = (2 * w + u) * 64 + lane, row = (id * 2185u) >> 16, rem = id - row * 30u, sec = (rem * 6554u) >> 16;
        if (2 * w + u < 15) glds(L_S0 + (2 * w + u) * 1024u, row * 3840u + sec * 1280u + (rem - sec * 10u) * 16u, srd, 0);
      }
    }
    const int J = r >= 20 ? (r - 20) * 8 + w : 96 + r * 8 + w;
    const unsigned sj = L_SJ + w * 6144u;
    if (J < 108) {
      const unsigned char* W = J < 20 ? wo : J < 40 ? w1 : J < 60 ? w2 : w4;
      const int s = J < 20 ? J : J < 40 ? J - 20 : J < 60 ? J - 40 : J - 60;
      const srd_t srd = mk_srd(W + 160 * c + (size_t)s * 40960, 40960 - 160 * c);
#pragma unroll
      for (int j = 0; j < 5; ++j) {
        const unsigned t = 64 * j + lane, row = (t * 6554u) >> 16;
        glds(sj + j * 1024u, row * 1280u + (t - row * 10u) * 16u, srd, 0);
      }
    } else if (J < 148) {
      const int s = (J - 108) >> 1, half = (J - 108) & 1;
      const srd_t srd = mk_srd(w3 + 384 * c + 192 * half + (size_t)s * 98304, 98304 - 384 * c - 192 * half);
#pragma unroll
      for (int j = 0; j < 6; ++j) {
        const unsigned t = 64 * j + lane, row = (t * 5462u) >> 16;
        glds(sj + j * 1024u, row * 3072u + (t - row * 12u) * 16u, srd, 0);
      }
    }
    if (FINE == 6) stamp(k, 121);
    {
      u64 e = __hip_atomic_load((GAS u64*)(wsp + WS_EPOCH), __ATOMIC_RELAXED, __HIP_MEMORY_SCOPE_AGENT);
      const unsigned lo = __builtin_amdgcn_readfirstlane((unsigned)e), hi = __builtin_amdgcn_readfirstlane((unsigned)(e >> 32));
      k.tag = ((u64)hi << 32) | lo;
    }
    v4u xin0[10];
    {
      const GAS v4u* px = (const GAS v4u*)(x + (size_t)(r * 64 + (tid >> 3)) * 1280 + (tid & 7) * 16);
#pragma unroll
      for (int i = 0; i < 10; ++i) xin0[i] = px[8 * i];
    }
    if (w == 7) {
      const srd_t srd = mk_srd(wphi + 3200 * c, 3200);
#pragma unroll
      for (int u = 0; u < 4; ++u) glds(L_SPH + u * 1024u, lane * 16 + u * 1024, srd, 0);
    }
    if (FINE == 6) { stamp(k, 122); WAIT_VM(0); stampw(k, 128); }
    if (FINE == 7 && tid == 0) *(LAS u64*)(k.L + L_MISC + 2816 + 150 * 8) = 0;
    BAR_VL0();
    asm volatile("" ::"v"(code_sink));
    stamp(k, 120);
    {
      const int row = lane & 15;
      LAS unsigned short* pw = (LAS unsigned short*)(k.L + L_SPH) + (w & 3) * 400;
#pragma unroll
      for (int fb = 0; fb < 2; ++fb) {
        const bool rowok = fb == 0 || (row & 3) == 0;
        const int f = 5 * (row >> 2) + (fb == 0 ? (row & 3) : 4);
        unsigned e[8];
#pragma unroll
        for (int j = 0; j < 8; ++j) {
          const bool ok = rowok && (j < 4 || g == 0);
          const int d = j < 4 ? 4 * g + j : 16 + (j - 4);
          const unsigned v = pw[(ok ? d : 0) * 20 + f];
          e[j] = ok ? v : 0u;
        }
        aphi[fb] = v4u{e[0] | (e[1] << 16), e[2] | (e[3] << 16), e[4] | (e[5] << 16), e[6] | (e[7] << 16)};
      }
    }
    if (r < 20) {
      const int j = w >> 1, role = w & 1, vqp = role == 0 ? p : (p == 1 ? 0 : 4);
      const v4u f0 = trfrag<3840>(k.L + L_S0 + trr * 480 + role * 160 + 40 * j + 8 * p);
      const v4u f1 = trfrag<3840>(k.L + L_S0 + trr * 480 + 40 * j + (p == 0 ? role * 160 + 32 : 320 + 8 * vqp));
      st128(f0, lane * 16, ws, wt + WT_A + r * 16384u + w * 2048u);
      st128(f1, lane * 16, ws, wt + WT_A + r * 16384u + w * 2048u + 1024u);
      chunk_sum(f0, ws, c, r, 2 * w, lane); chunk_sum(f1, ws, c, r, 2 * w + 1, lane);
    }
    if (J < 108) {
      const int s = J < 20 ? J : J < 40 ? J - 20 : J < 60 ? J - 40 : J - 60;
      const int n = s;
      const unsigned dst = wt + (J < 20 ? WT_O : J < 40 ? WT_1 : J < 60 ? WT_2 : WT_4) + n * 5120u;
#pragma unroll
      for (int bl = 0; bl < 5; ++bl) {
        const v4u f = trfrag<1280>(k.L + sj + trr * 160 + p * 8 + 32 * bl);
        st128(f, lane * 16, ws, dst + bl * 1024u);
        if (J >= 20 && J < 40) chunk_sum(f, ws, c, s, 16 + bl, lane);
      }
    } else if (J < 148) {
      const int s = (J - 108) >> 1, half = (J - 108) & 1;
#pragma unroll
      for (int bl = 0; bl < 6; ++bl) {
        const v4u f = trfrag<1536>(k.L + sj + trr * 192 + p * 8 + 32 * bl);
        st128(f, lane * 16, ws, wt + WT_3 + s * 12288u + (6 * half + bl) * 1024u);
        chunk_sum(f, ws, c, s, 21 + 6 * half + bl, lane);
      }
    }
    BAR_VL0();
    post_flag(k, fl_col(PH_T, r, c), tag_of(k, PH_T));
    if (w >= 4) { poison_tile(k, ws, WS_ACT + A_O, -1, w - 4, lane); poison_hs(k, ws, WS_ACT, w - 4, lane); }
    if (FINE == 6) stamp(k, 123);
    {
      const int tok = tid >> 3, part = tid & 7;
      float s = 0.f, q = 0.f;
#pragma unroll
      for (int i = 0; i < 10; ++i) {
        const int u = part + 8 * i;
        const int B = u >> 1, gq = (u & 1) * 2;
        LAS unsigned char* q0 = k.L + L_X + ((B >> 1) * 4 + (tok >> 4)) * 1024 + (tok & 15) * 16 + (B & 1) * 512 + (gq >> 1) * 8;
        *(LAS v2u*)(q0) = v2u{xin0[i].x, xin0[i].y};
        *(LAS v2u*)(q0 + 256) = v2u{xin0[i].z, xin0[i].w};
#pragma unroll
        for (int e = 0; e < 4; ++e) {
          const float a = bflo(xin0[i][e]), bb = bfhi(xin0[i][e]);
          s += a + bb; q = __builtin_fmaf(a, a, q); q = __builtin_fmaf(bb, bb, q);
        }
      }
      s += __shfl_xor(s, 1); q += __shfl_xor(q, 1); s += __shfl_xor(s, 2); q += __shfl_xor(q, 2); s += __shfl_xor(s, 4); q += __shfl_xor(q, 4);
      const float mean = s * (1.0f / 640.0f), var = __builtin_fmaxf(__builtin_fmaf(-mean, mean, q * (1.0f / 640.0f)), 0.0f);
      if (part == 0) *(LAS f32x2*)(k.L + L_STATS + tok * 8) = f32x2{mean, __builtin_amdgcn_rsqf(var + 1e-5f)};
    }
    BAR_L();
#pragma unroll
    for (int j = 0; j < 5; ++j) {
      const int B = 5 * c + j;
      xres[j] = unpk(*(LAS v2u*)(k.L + L_X + ((B >> 1) * 4 + (w & 3)) * 1024 + (2 * (B & 1) + (g & 1)) * 256 + (lane & 15) * 16 + (g >> 1) * 8));
    }
  }
  stamp(k, 1);

  f32x4 x2r[5], x1r[5];

#pragma unroll 1
  for (int layer = 0; layer < 2; ++layer) {
    {
      PHASE_VARS()
      const int lane = opq(lane0), t = lane & 15, g = lane >> 4, j = w & 3, role = w < 4 ? 1 : 0;
      const unsigned v16 = pinv(lane * 16);
      const unsigned wa = pin(wt + WT_A + j * 4096u);
#define A_WL1(i, f) ld128<(f) * 1024>(wr[(i) % 6][f], v16, ws, wa + (i) * 16384u);
#define A_WLOAD(i) { A_WL1(i, 0) A_WL1(i, 1) A_WL1(i, 2) A_WL1(i, 3) }
      const unsigned xa = pin((unsigned)(w < 4 ? w : w + 6) * 4096u), xb2 = pin((unsigned)(w < 4 ? 8 + (w >> 1) : 16 + (w >> 1)) * 4096u + (w & 1) * 2048u);
      const unsigned xsrc = pin(WS_ACT + A_XO + (unsigned)r * 81920u);
      if (ly == 0) { poll_flags(k, fl_col(PH_T, lane & 31, c), tag_of(k, PH_T)); BAR(); }
      v4u bfr[4];
      if (w < 4) {
        v4u wr[6][4];
        A_WLOAD(0) A_WLOAD(1)
        if (ly != 0) {
          BAR_L();
          __builtin_amdgcn_s_sleep(XDELAY);
          FST(42)
          gln<4>(L_X + xa, v16, ws, xsrc + xa); gln<4>(L_X + xa + 16384u, v16, ws, xsrc + xa + 16384u); gln<2>(L_X + xb2, v16, ws, xsrc + xb2);
          WAIT_VM(6); ldscheck<4>(k, L_X + xa, v16, ws, xsrc + xa);
          BAR();
        } else {
          if (tid < 132) {
            v4u pc[20];
#pragma unroll
            for (int s2 = 0; s2 < 20; ++s2) pc[s2] = __builtin_amdgcn_raw_buffer_load_b128(ws, tid * 16, WS_PCS + (unsigned)c * PCS_C + s2 * 33 * 64, 16);
            SB();
            f32x4 a = {0.f, 0.f, 0.f, 0.f};
#pragma unroll
            for (int s2 = 0; s2 < 20; ++s2) a += __builtin_bit_cast(f32x4, pc[s2]);
            *(LAS f32x4*)(k.L + L_CSL + tid * 16) = a;
          }
          BAR_VL0();
        }
        A_WLOAD(2) A_WLOAD(3) A_WLOAD(4)
        stamp(k, sb + 0);
        f32x4 acc[4][4];
        acc_zero(acc[0]); acc_zero(acc[1]); acc_zero(acc[2]); acc_zero(acc[3]);
        v4u xf[2][4], xn[4];
        LAS unsigned char *xb = lds_at(L_X + lane * 16), *xb2l = lds_at(L_X + 65536 + lane * 16);
#define A_IT(i, N)                                                                                    \
  {                                                                                                   \
    asm volatile("s_waitcnt vmcnt(%4)" : "+v"(wr[(i) % 6][0]), "+v"(wr[(i) % 6][1]), "+v"(wr[(i) % 6][2]), "+v"(wr[(i) % 6][3]) : "i"(N)); \
    if ((i) == 3) {                                 \
      if (ly != 0) { ldscheck<4>(k, L_X + xa + 16384u, v16, ws, xsrc + xa + 16384u); ldscheck<2>(k, L_X + xb2, v16, ws, xsrc + xb2); } \
      BAR_L();                                                                                        \
    }                                                                                                 \
    _Pragma("unroll") for (int tb = 0; tb < 4; ++tb) {                                                \
      mfma_ip(acc[0][tb], wr[(i) % 6][0], xf[(i) & 1][tb]);                                           \
      mfma_ip(acc[1][tb], wr[(i) % 6][1], xf[(i) & 1][tb]);                                           \
      if ((i) + 1 < 20) xn[tb] = *(LAS v4u*)(((i) + 1 < 16 ? xb : xb2l) + (((i) + 1) % 16 * 4 + tb) * 1024); \
      mfma_ip(acc[2][tb], wr[(i) % 6][2], xf[(i) & 1][tb]);                                           \
      mfma_ip(acc[3][tb], wr[(i) % 6][3], xf[(i) & 1][tb]);                                           \
      if ((i) + 5 < 20) { if (tb == 0) A_WL1((i) + 5, 0) if (tb == 1) A_WL1((i) + 5, 1) if (tb == 2) A_WL1((i) + 5, 2) if (tb == 3) A_WL1((i) + 5, 3) } \
    }                                                                                                 \
    if ((i) + 1 < 20) { _Pragma("unroll") for (int tb = 0; tb < 4; ++tb) xf[((i) + 1) & 1][tb] = xn[tb]; } \
  }
        FST(20)
#pragma unroll
        for (int tb = 0; tb < 4; ++tb) xf[0][tb] = *(LAS v4u*)(xb + tb * 1024);
        A_IT(0, 63) A_IT(1, 63) A_IT(2, 16) A_IT(3, 16) FST(21) A_IT(4, 16) A_IT(5, 16) A_IT(6, 16) A_IT(7, 16) FST(22)
        A_IT(8, 16) A_IT(9, 16) A_IT(10, 16) A_IT(11, 16) FST(23) A_IT(12, 16) A_IT(13, 16) A_IT(14, 16) A_IT(15, 16) FST(24)
        A_IT(16, 12) A_IT(17, 8) A_IT(18, 4) A_IT(19, 0)
#undef A_IT
        acc_fence(acc[0]); acc_fence(acc[1]); acc_fence(acc[2]); acc_fence(acc[3]);
        BAR_L();
        stamp(k, sb + 1);
        LAS unsigned char* img = k.L + L_IMG + j * IMG_H;
        LAS unsigned char* hq = k.L + 28 * SLOT + j * 2560;
        const f32x4 cq0 = *(LAS f32x4*)(k.L + L_CSL + (4 * j) * 64 + g * 16), cq1 = *(LAS f32x4*)(k.L + L_CSL + (4 * j + 1) * 64 + g * 16);
        const f32x4 ck0 = *(LAS f32x4*)(k.L + L_CSL + (4 * j + 2) * 64 + g * 16), ck1 = *(LAS f32x4*)(k.L + L_CSL + (4 * j + 3) * 64 + g * 16);
        auto fold = [](f32x4 a, f32x4 cs, float nm, float rs) {
          return v2u{pk2v((f32x2{cs.x, cs.y} * nm + f32x2{a.x, a.y}) * rs), pk2v((f32x2{cs.z, cs.w} * nm + f32x2{a.z, a.w}) * rs)};
        };
#pragma unroll
        for (int tb = 0; tb < 4; ++tb) {
          const f32x2 ms = *(LAS f32x2*)(k.L + L_STATS + (tb * 16 + t) * 8);
          const v2u q0 = fold(acc[0][tb], cq0, -ms.x, ms.y), q1 = fold(acc[1][tb], cq1, -ms.x, ms.y);
          const v2u k0 = fold(acc[2][tb], ck0, -ms.x, ms.y), k1 = fold(acc[3][tb], ck1, -ms.x, ms.y);
          *(LAS v2u*)(hq + tb * 640 + lane * 8) = q0;
          if (g == 0) *(LAS v2u*)(hq + tb * 640 + 512 + t * 8) = q1;
          LAS unsigned char* vrow = img + (tb * 16 + t) * 160 + 96;
          if (g != 0) *(LAS v2u*)(vrow + 8 * g) = q1;
          *(LAS v2u*)(vrow + 8 * (g == 0 ? 7 : g == 1 ? 0 : g == 2 ? 4 : 5)) = g == 3 ? v2u{0x00003F80u, 0u} : k1;
          bfr[tb] = v4u{k0.x, k0.y, k1.x, k1.y};
        }
        BAR_L();
      } else {
        if (ly != 0) {
          BAR_L();
          __builtin_amdgcn_s_sleep(XDELAY);
          gln<4>(L_X + xa, v16, ws, xsrc + xa); gln<4>(L_X + xa + 16384u, v16, ws, xsrc + xa + 16384u); gln<2>(L_X + xb2, v16, ws, xsrc + xb2);
          BAR();
          StatLd st;
          stat_issue(st, ws, 2, r * 4 + j, lane);
          WAIT_VM(0);
          ldscheck<4>(k, L_X + xa, v16, ws, xsrc + xa); ldscheck<4>(k, L_X + xa + 16384u, v16, ws, xsrc + xa + 16384u);
          ldscheck<2>(k, L_X + xb2, v16, ws, xsrc + xb2);
          wait_tie_stat<0>(st);
          statcheck(st, ws, 2, r * 4 + j, lane);
          float mean, var; stat_finish(st, mean, var);
          if (lane < 16) *(LAS f32x2*)(k.L + L_STATS + (j * 16 + lane) * 8) = f32x2{mean, __builtin_amdgcn_rsqf(var + 1e-5f)};
        } else {
          BAR_VL0();
          post_flag(k, fl_group(PH_TG, r, c), tag_of(k, PH_TG));
        }
        BAR_L();
        hstream(k, ws, wt + WT_O + 40 * 1024, P_C * 5 + 40, 60, v16);
        BAR_L();
        BAR_L();
        LAS unsigned char* hq = k.L + 28 * SLOT + j * 2560;
#pragma unroll
        for (int tb = 0; tb < 4; ++tb) {
          const v2u q0 = *(LAS v2u*)(hq + tb * 640 + lane * 8), q1 = *(LAS v2u*)(hq + tb * 640 + 512 + t * 8);
          bfr[tb] = v4u{q0.x, q0.y, g == 0 ? q1.x : 0u, g == 0 ? q1.y : 0u};
        }
      }
#undef A_WLOAD
#undef A_WL1
      unsigned phq[4][5];
      LAS unsigned char* img = k.L + L_IMG + j * IMG_H;
      {
        f32x4 pa[4], pb[4];
        const f32x4 z = {0.f, 0.f, 0.f, 0.f};
#pragma unroll
        for (int tb = 0; tb < 4; ++tb) { pa[tb] = mfma(aphi[0], bfr[tb], z); pb[tb] = mfma(aphi[1], bfr[tb], z); }
        SB();
        constexpr float L2E = 1.4426950408889634f;
        f32x2 p01[4], p23[4]; float p4[4], mx[4], mn[4];
#pragma unroll
        for (int tb = 0; tb < 4; ++tb) {
          p01[tb] = f32x2{pa[tb].x, pa[tb].y} * L2E; p23[tb] = f32x2{pa[tb].z, pa[tb].w} * L2E; p4[tb] = pb[tb].x * L2E;
          mx[tb] = fmax3_raw(fmax3_raw(p01[tb].x, p01[tb].y, p23[tb].x), p23[tb].y, p4[tb]);
          mn[tb] = -fmin3_raw(fmin3_raw(p01[tb].x, p01[tb].y, p23[tb].x), p23[tb].y, p4[tb]);
        }
        xr4<true>(mx, mn);
        SB();
        float e[4][5], f[4][5], sp[4], sm[4];
#pragma unroll
        for (int tb = 0; tb < 4; ++tb) {
          const f32x2 a = p01[tb] - mx[tb], b = p23[tb] - mx[tb], cc = p01[tb] + mn[tb], d = p23[tb] + mn[tb];
          e[tb][0] = __builtin_amdgcn_exp2f(a.x); e[tb][1] = __builtin_amdgcn_exp2f(a.y); e[tb][2] = __builtin_amdgcn_exp2f(b.x);
          e[tb][3] = __builtin_amdgcn_exp2f(b.y); e[tb][4] = __builtin_amdgcn_exp2f(p4[tb] - mx[tb]);
          f[tb][0] = __builtin_amdgcn_exp2f(-cc.x); f[tb][1] = __builtin_amdgcn_exp2f(-cc.y); f[tb][2] = __builtin_amdgcn_exp2f(-d.x);
          f[tb][3] = __builtin_amdgcn_exp2f(-d.y); f[tb][4] = __builtin_amdgcn_exp2f(-(p4[tb] + mn[tb]));
        }
#pragma unroll
        for (int tb = 0; tb < 4; ++tb) {
          const f32x2 s2 = (f32x2{e[tb][0], e[tb][1]} + f32x2{e[tb][2], e[tb][3]}), s3 = (f32x2{f[tb][0], f[tb][1]} + f32x2{f[tb][2], f[tb][3]});
          sp[tb] = (s2.x + s2.y) + e[tb][4]; sm[tb] = (s3.x + s3.y) + f[tb][4];
        }
        xr4<false>(sp, sm);
        SB();
#pragma unroll
        for (int tb = 0; tb < 4; ++tb) {
          const float rp = __builtin_amdgcn_rcpf(sp[tb]), rm = __builtin_amdgcn_rcpf(sm[tb]);
          phq[tb][0] = pk2v(f32x2{e[tb][0], e[tb][1]} * rp); phq[tb][1] = pk2v(f32x2{e[tb][2], e[tb][3]} * rp);
          phq[tb][2] = pk2(e[tb][4] * rp, f[tb][0] * rm);
          phq[tb][3] = pk2v(f32x2{f[tb][1], f[tb][2]} * rm); phq[tb][4] = pk2v(f32x2{f[tb][3], f[tb][4]} * rm);
        }
        if (role == 1) {
          LAS unsigned char* row = img + t * 160 + 24 * g;
#pragma unroll
          for (int tb = 0; tb < 4; ++tb) {
            *(LAS v2u*)(row + tb * 2560) = v2u{phq[tb][0], phq[tb][1]};
            *(LAS v2u*)(row + tb * 2560 + 8) = v2u{phq[tb][2], phq[tb][3]};
            *(LAS v2u*)(row + tb * 2560 + 16) = v2u{phq[tb][4], 0u};
          }
        }
      }
      FST(25)
      if (role == 1) {
        asm volatile("s_waitcnt lgkmcnt(0)" ::: "memory");
        const unsigned lb = tr_row(lane) * 160 + (lane & 3) * 8;
        f32x4 kv[2][3];
#pragma unroll
        for (int cb = 0; cb < 3; ++cb) { kv[0][cb] = f32x4{0.f, 0.f, 0.f, 0.f}; kv[1][cb] = f32x4{0.f, 0.f, 0.f, 0.f}; }
#pragma unroll
        for (int ks = 0; ks < 2; ++ks) {
          const v4u bv0 = trfrag<1280>(img + lb + ks * 5120 + 96), bv1 = trfrag<1280>(img + lb + ks * 5120 + 128);
#pragma unroll
          for (int cb = 0; cb < 3; ++cb) {
            const v4u a = trfrag<1280>(img + lb + ks * 5120 + 32 * cb);
            kv[0][cb] = mfma(a, bv0, kv[0][cb]); kv[1][cb] = mfma(a, bv1, kv[1][cb]);
          }
        }
        const unsigned so = WS_PART + ly * PART_L + (unsigned)(c * 32 + r) * 16384u + j * 4096u;
        const unsigned vo2 = t < 5 ? (unsigned)(3072 + (g * 5 + t) * 16) : OOB;
        const u64 tg = tag_of(k, ph0 + PH_KV1);
#pragma unroll
        for (int cb = 0; cb < 3; ++cb) { st128(granule(kv[0][cb], tg), v16, ws, so + cb * 1024u); st128(granule(kv[1][cb], tg), vo2, ws, so + cb * 320u); }
      }
      FST(26)
      stamp(k, sb + 2);
      if (w == 1 && ly == 0) poll_flags_any(k, fl_group(PH_TG, r, lane & 7), tag_of(k, PH_TG));
      if (w >= 1 && w < 4) {
#pragma unroll 1
        for (int g4 = (w - 1) * 4; g4 < 40; g4 += 12) gln<4>((unsigned)(P_C * 5 + g4) * 1024u, v16, ws, wt + WT_O + (unsigned)g4 * 1024u);
      }
      if (w == 0) {
        const unsigned so = WS_PART + ly * PART_L + (unsigned)(c * 32) * 16384u + r * 512u;
        const unsigned vo2 = (unsigned)((lane >> 5) * 16384 + (lane & 31) * 16);
        const bool hole = (r & 7) == 7 && (lane & 31) >= 28;
        const u64 tg = tag_of(k, ph0 + PH_KV1);
        v4u pr[16];
        if (FINE == 4 && ly == 1) stampw(k, 128);
        unsigned spins = 0;
        for (;;) {
          bool ok = true;
#pragma unroll
          for (int i = 0; i < 16; ++i) pr[i] = __builtin_amdgcn_raw_buffer_load_b128(ws, vo2, so + i * 32768u, 16);
          SB();
#pragma unroll
          for (int i = 0; i < 16; ++i) ok = ok && granule_ok(pr[i], tg);
          if (__all(ok || hole) || !spin_more(k, spins)) break;
        }
        if (FINE == 4 && ly == 1) { stampw(k, 136); if (lane == 0) *(LAS u64*)(k.L + L_MISC + 2816 + 152 * 8) = spins; }
        f32x4 sum = {0.f, 0.f, 0.f, 0.f};
#pragma unroll
        for (int i = 0; i < 16; ++i) sum += f32x4{bflo(pr[i].x), bfhi(pr[i].x), bflo(pr[i].z), bfhi(pr[i].z)};
#pragma unroll
        for (int e = 0; e < 4; ++e) {
          auto sw = __builtin_amdgcn_permlane32_swap(asu(sum[e]), asu(sum[e]), false, false);
          sum[e] = asf(sw[0]) + asf(sw[1]);
        }
        st128(granule(sum, tag_of(k, ph0 + PH_KV2)), lane < 32 ? v16 : OOB, ws, WS_RED + (unsigned)(ly * 8 + c) * 16384u + r * 512u);
        stamp(k, sb + 3);
      } else if (role == 0) {
        const unsigned so = WS_RED + (unsigned)(ly * 8 + c) * 16384u + j * 4096u;
        const int dd = t < 4 ? t : ((t & 3) == 0 ? 4 : -1);
        const int Q0 = 20 * c + 5 * j + g, Q1 = 20 * c + 5 * j + 4;
        const unsigned o0 = pinv(frag_off(Q0 >> 2, Q0 & 3, t)), o1 = pinv(g == 0 ? frag_off(Q1 >> 2, Q1 & 3, t) : OOB);
        const u64 tg = tag_of(k, ph0 + PH_KV2);
        poison_tile(k, ws, act + A_X2, ly * 3 + 0, j, lane);
        v4u l0[3], l1[3];
        if (FINE == 4 && ly == 1) stampw(k, 128);
        unsigned spins = 0;
        for (;;) {
          bool ok = true;
#pragma unroll
          for (int u = 0; u < 3; ++u) {
            const int qc = 3 * g + u;
            l0[u] = __builtin_amdgcn_raw_buffer_load_b128(ws, (unsigned)((qc >> 2) * 1024 + ((qc & 3) * 16 + t) * 16), so, 16);
            l1[u] = __builtin_amdgcn_raw_buffer_load_b128(ws, dd >= 0 ? (unsigned)(3072 + (qc * 5 + dd) * 16) : OOB, so, 16);
          }
          SB();
#pragma unroll
          for (int u = 0; u < 3; ++u) ok = ok && granule_ok(l0[u], tg) && (dd < 0 || granule_ok(l1[u], tg));
          if (__all(ok) || !spin_more(k, spins)) break;
        }
        if (FINE == 4 && ly == 1) { stampw(k, 136); if (lane == 0 && w == 0) *(LAS u64*)(k.L + L_MISC + 2816 + 153 * 8) = spins; }
        stamp(k, sb + 3);
        const v4u a00 = {l0[0].x, l0[0].z, l0[1].x, l0[1].z}, a01 = {l0[2].x, l0[2].z, 0u, 0u};
        const v4u a10 = {l1[0].x, l1[0].z, l1[1].x, l1[1].z}, a11 = {l1[2].x, l1[2].z, 0u, 0u};
        f32x4 n0[4], n1[4];
        const f32x4 z = {0.f, 0.f, 0.f, 0.f};
#pragma unroll
        for (int tb = 0; tb < 4; ++tb) {
          const v4u b0 = {phq[tb][0], phq[tb][1], phq[tb][2], phq[tb][3]}, b1 = {phq[tb][4], 0u, 0u, 0u};
          n0[tb] = mfma(a01, b1, mfma(a00, b0, z)); n1[tb] = mfma(a11, b1, mfma(a10, b0, z));
        }
#pragma unroll
        for (int tb = 0; tb < 4; ++tb) {
          auto sw = __builtin_amdgcn_permlane16_swap(asu(n1[tb].x), asu(n1[tb].x), false, false);
          const float rd = __builtin_amdgcn_rcpf((g == 0 ? asf(sw[1]) : n1[tb].x) + 1e-6f);
          const unsigned so2 = act + A_O + (unsigned)r * 81920u + tb * 1024u;
          st64(canon2(pk4(n0[tb] * rd)), o0, ws, so2);
          st64(canon2(pk4(n1[tb] * rd)), o1, ws, so2);
        }
      }
      if (w >= 1 && w < 4) WAIT_VM(0);
      BAR();
      stamp(k, sb + 4);
    }
    if (STOP_PHASE == layer * 7 + 0) return;
    {
      PHASE_VARS()
      const int lane = opq(lane0);
      const unsigned v16 = pinv(lane * 16), xo = kx_off(k, act + A_O);
      const Ring rg = mk_ring(lane);
      f32x4 acc[5];
      if (w < 4) {
        v4u xf[20];
        __builtin_amdgcn_s_sleep(XDELAY);
        kx_issue(xf, ws, xo, v16);
        stamp(k, sb + 5);
        acc_zero(acc);
        KMAIN(P_C, 0, true)
      } else {
        hstream(k, ws, wt + WT_1, P_D * 5, 15, v16);
        hstream(k, ws, wt + WT_1 + 15 * 1024, 0, 40, v16);
        poison_tile(k, ws, act + A_X1, ly * 3 + 1, w - 4, lane);
        undef5(acc);
      }
      BAR_L();
      stamp(k, sb + 6);
      if (w < 4) {
        v2u pk[5];
#pragma unroll
        for (int j = 0; j < 5; ++j) { pk[j] = pk4(xres[j] + rnd4(acc[j])); x2r[j] = unpk(pk[j]); }
        kstore(k, ws, act + A_X2, pk, lane);
        kstats(k, ws, ly * 3 + 0, x2r, lane);
        FST(28)
      } else {
        hstream(k, ws, wt + WT_1 + 55 * 1024, 40, 45, v16);
        undef5(x2r);
      }
      stamp(k, sb + 7);
    }
    if (STOP_PHASE == layer * 7 + 1) return;
    {
      PHASE_VARS()
      const int lane = opq(lane0), t = lane & 15, g = lane >> 4;
      const unsigned v16 = pinv(lane * 16), xo = kx_off(k, act + A_X2);
      const Ring rg = mk_ring(lane);
      const int shift = (c >> 1) == 0 ? 1 : (c >> 1) == 1 ? -1 : (c >> 1) == 2 ? 2 : -2;
      const int tok = r * 64 + (w & 3) * 16 + t, dst = tok + shift;
      const unsigned dof = pinv((dst >= 0 && dst < S_) ? (unsigned)(dst >> 6) * 81920u + (unsigned)((dst >> 4) & 3) * 1024u + frag_off(0, g, dst & 15) : OOB);
      f32x4 acc[5];
      StatLd st;
      if (w < 4) {
        v4u xf[20];
        __builtin_amdgcn_s_sleep(XDELAY);
        kx_issue(xf, ws, xo, v16);
        stat_issue(st, ws, ly * 3 + 0, r * 4 + w, lane);
        BAR();
        stamp(k, sb + 8);
        acc_zero(acc);
        KMAIN(P_D, 2, true)
        wait_tie_stat<0>(st);
        statcheck(st, ws, ly * 3 + 0, r * 4 + w, lane);
      } else {
        WAIT_VM(0);
        BAR();
        hstream(k, ws, wt + WT_2, P_E * 5, 55, v16);
        poison_h2(k, ws, act, w - 4, lane);
        undef5(acc); asm volatile("" : "=v"(st.s0), "=v"(st.s1));
      }
      BAR_L();
      stamp(k, sb + 9);
      if (w < 4) {
        float mean, var; stat_finish(st, mean, var);
        const float rstd = __builtin_amdgcn_rsqf(var + 1e-5f);
        const float sc2 = rstd * __builtin_amdgcn_rsqf(var * rstd * rstd + 1e-5f);
#pragma unroll
        for (int j = 0; j < 5; ++j)
          st64(canon2(ln_gelu4(acc[j], *(LAS f32x4*)(k.L + L_CSL + (16 + j) * 64 + g * 16), -mean, sc2)), dof, ws, act + A_HS + blk_off(5 * c + j));
        if (r == 0 || r == 31) {
          const bool z = shift > 0 ? tok < shift : tok >= S_ + shift;
          const unsigned zof = z ? (unsigned)(tok >> 6) * 81920u + (unsigned)((tok >> 4) & 3) * 1024u + frag_off(0, g, tok & 15) : OOB;
#pragma unroll
          for (int j = 0; j < 5; ++j) st64(v2u{0u, 0u}, zof, ws, act + A_HS + blk_off(5 * c + j));
        }
#pragma unroll
        for (int j = 0; j < 5; ++j) x1r[j] = rnd4((x2r[j] - mean) * rstd);
      } else {
        hstream(k, ws, wt + WT_2 + 55 * 1024, 140, 15, v16);
        hstream(k, ws, wt + WT_2 + 70 * 1024, 0, 30, v16);
        undef5(x1r);
      }
      stamp(k, sb + 10);
    }
    if (STOP_PHASE == layer * 7 + 2) return;
    {
      PHASE_VARS()
      const int lane = opq(lane0);
      const unsigned v16 = pinv(lane * 16), xo = kx_off(k, act + A_HS);
      const Ring rg = mk_ring(lane);
      f32x4 acc[5];
      if (w < 4) {
        v4u xf[20];
        __builtin_amdgcn_s_sleep(XDELAY);
        kx_issue(xf, ws, xo, v16);
        BAR();
        stamp(k, sb + 11);
        acc_zero(acc);
        KMAIN(P_E, 0, true)
      } else {
        WAIT_VM(0);
        BAR();
        hstream(k, ws, wt + WT_4, P_G * 5, 45, v16);
        if (ly == 0) poison_tile(k, ws, act + A_XO, 2, w - 4, lane);
        undef5(acc);
      }
      stamp(k, sb + 12);
      if (w < 4) {
        v2u pk[5];
#pragma unroll
        for (int j = 0; j < 5; ++j) { pk[j] = pk4(x1r[j] + rnd4(acc[j])); x1r[j] = unpk(pk[j]); }
        kstore(k, ws, act + A_X1, pk, lane);
        kstats(k, ws, ly * 3 + 1, x1r, lane);
      } else {
        undef5(x1r);
      }
      stamp(k, sb + 13);
    }
    if (STOP_PHASE == layer * 7 + 3) return;
    {
      PHASE_VARS()
      const int lane = opq(lane0), t = lane & 15, g = lane >> 4, cp = w & 3, kh = w >> 2;
      const unsigned v16 = pinv(lane * 16);
      const unsigned wa3 = pin(wt + WT_3 + cp * 3072u);
#define F_WL1(i, f) ld128<(f) * 1024>(wf3[(i) % 5][f], v16, ws, wa3 + (i) * 12288u);
#define F_WLOAD(i) { F_WL1(i, 0) F_WL1(i, 1) F_WL1(i, 2) }
      const unsigned xa = pin((unsigned)(w < 4 ? w : w + 6) * 4096u), xb2 = pin((unsigned)(w < 4 ? 8 + (w >> 1) : 16 + (w >> 1)) * 4096u + (w & 1) * 2048u);
      const unsigned xsrc = pin(act + A_X1 + (unsigned)r * 81920u);
      f32x4 fin[3][2];
      if (w < 4) {
        v4u wf3[5][3];
        F_WLOAD(0)
        BAR_L();
        __builtin_amdgcn_s_sleep(XDELAY);
        FST(41)
        gln<4>(L_XF + xa, v16, ws, xsrc + xa); gln<4>(L_XF + xa + 16384u, v16, ws, xsrc + xa + 16384u); gln<2>(L_XF + xb2, v16, ws, xsrc + xb2);
        f32x4 acc[3][4];
        acc_zero(acc[0]); acc_zero(acc[1]); acc_zero(acc[2]);
        WAIT_VM(6); ldscheck<4>(k, L_XF + xa, v16, ws, xsrc + xa);
        BAR();
        F_WLOAD(1) F_WLOAD(2) F_WLOAD(3)
        stamp(k, sb + 14);
        v4u xf[2][4], xn[4];
        LAS unsigned char *xb = lds_at(L_XF + lane * 16), *xb2l = lds_at(L_XF + 65536 + lane * 16);
#define F_IT(i, N)                                                                                    \
  {                                                                                                   \
    wait_tie3<N>(wf3[(i) % 5][0], wf3[(i) % 5][1], wf3[(i) % 5][2]);                                  \
    if ((i) == 3) {                                 \
      ldscheck<4>(k, L_XF + xa + 16384u, v16, ws, xsrc + xa + 16384u); ldscheck<2>(k, L_XF + xb2, v16, ws, xsrc + xb2); \
      BAR_L();                                                                                        \
    }                                                                                                 \
    _Pragma("unroll") for (int tb = 0; tb < 4; ++tb) {                                                \
      mfma_ip(acc[0][tb], wf3[(i) % 5][0], xf[(i) & 1][tb]);                                          \
      mfma_ip(acc[1][tb], wf3[(i) % 5][1], xf[(i) & 1][tb]);                                          \
      if ((i) + 1 < 20) xn[tb] = *(LAS v4u*)(((i) + 1 < 16 ? xb : xb2l) + (((i) + 1) % 16 * 4 + tb) * 1024); \
      mfma_ip(acc[2][tb], wf3[(i) % 5][2], xf[(i) & 1][tb]);                                          \
      if ((i) + 4 < 20) { if (tb == 0) F_WL1((i) + 4, 0) if (tb == 1) F_WL1((i) + 4, 1) if (tb == 2) F_WL1((i) + 4, 2) } \
    }                                                                                                 \
    if ((i) + 1 < 20) { _Pragma("unroll") for (int tb = 0; tb < 4; ++tb) xf[((i) + 1) & 1][tb] = xn[tb]; } \
  }
        FST(29)
#pragma unroll
        for (int tb = 0; tb < 4; ++tb) xf[0][tb] = *(LAS v4u*)(xb + tb * 1024);
        F_IT(0, 63) F_IT(1, 9) FST(30) F_IT(2, 9) F_IT(3, 9) FST(31) F_IT(4, 9) F_IT(5, 9) F_IT(6, 9) F_IT(7, 9) F_IT(8, 9) F_IT(9, 9) FST(32)
        F_IT(10, 9) F_IT(11, 9) F_IT(12, 9) F_IT(13, 9) F_IT(14, 9) F_IT(15, 9) F_IT(16, 9) F_IT(17, 6) F_IT(18, 3) F_IT(19, 0)
#undef F_IT
        acc_fence(acc[0]); acc_fence(acc[1]); acc_fence(acc[2]);
        BAR_L();
        stamp(k, sb + 15);
        LAS f32x4* sc = (LAS f32x4*)(k.L + L_XF + cp * 6144 + v16);
#pragma unroll
        for (int bl = 0; bl < 3; ++bl) { sc[(bl * 2) * 64] = acc[bl][2]; sc[(bl * 2 + 1) * 64] = acc[bl][3]; fin[bl][0] = acc[bl][0]; fin[bl][1] = acc[bl][1]; }
        BAR_L();
      } else {
        BAR_L();
        __builtin_amdgcn_s_sleep(XDELAY);
        gln<4>(L_XF + xa, v16, ws, xsrc + xa); gln<4>(L_XF + xa + 16384u, v16, ws, xsrc + xa + 16384u); gln<2>(L_XF + xb2, v16, ws, xsrc + xb2);
        BAR();
        StatLd st;
        stat_issue(st, ws, ly * 3 + 1, r * 4 + cp, lane);
        WAIT_VM(0);
        ldscheck<4>(k, L_XF + xa, v16, ws, xsrc + xa); ldscheck<4>(k, L_XF + xa + 16384u, v16, ws, xsrc + xa + 16384u);
        ldscheck<2>(k, L_XF + xb2, v16, ws, xsrc + xb2);
        wait_tie_stat<0>(st);
        statcheck(st, ws, ly * 3 + 1, r * 4 + cp, lane);
        float mean, var; stat_finish(st, mean, var);
        if (lane < 16) *(LAS f32x2*)(k.L + L_STATS + (cp * 16 + lane) * 8) = f32x2{mean, __builtin_amdgcn_rsqf(var + 1e-5f)};
        BAR_L();
        hstream(k, ws, wt + WT_4 + 125 * 1024, 0, 30, v16);
        BAR_L();
        BAR_L();
        LAS f32x4* sc = (LAS f32x4*)(k.L + L_XF + cp * 6144 + v16);
#pragma unroll
        for (int bl = 0; bl < 3; ++bl) { fin[bl][0] = sc[(bl * 2) * 64]; fin[bl][1] = sc[(bl * 2 + 1) * 64]; }
      }
#undef F_WLOAD
#undef F_WL1
      FST(33)
      const unsigned lo = frag_off(0, g, t);
#pragma unroll
      for (int u = 0; u < 2; ++u) {
        const int tb = 2 * kh + u;
        const f32x2 ms = *(LAS f32x2*)(k.L + L_STATS + (tb * 16 + t) * 8);
#pragma unroll
        for (int bl = 0; bl < 3; ++bl) {
          const int B = 12 * c + 3 * cp + bl;
          st64(canon2(ln_gelu4(fin[bl][u], *(LAS f32x4*)(k.L + L_CSL + (21 + 3 * cp + bl) * 64 + g * 16), -ms.x, ms.y)), lo, ws,
               act + A_H2 + (unsigned)r * 196608u + tb * 1024u + blk_off(B));
        }
      }
      FST(34)
      BAR_L();
      stamp(k, sb + 16);
      hstream(k, ws, wt + WT_4 + 45 * 1024, 75, 40, v16);
    }
    if (STOP_PHASE == layer * 7 + 4) return;
    {
      PHASE_VARS()
      const int lane = opq(lane0), t = lane & 15, g = lane >> 4;
      const unsigned v16 = pinv(lane * 16);
      const unsigned xo = pin(act + A_H2 + (unsigned)r * 196608u + (unsigned)(w & 3) * 1024u);
      const Ring rg = mk_ring(lane);
      f32x4 acc[5];
      if (w < 4) {
        __builtin_amdgcn_s_sleep(GDELAY);
        stamp(k, sb + 17);
        v4u xf[16];
#define GX(i) ld128<0>(xf[(i) & 15], v16, ws, xo + (i) * 4096u);
        GX(0) GX(1) GX(2) GX(3) GX(4) GX(5) GX(6) GX(7) GX(8) GX(9) GX(10) GX(11) GX(12) GX(13) GX(14) GX(15)
        acc_zero(acc);
        v4u wf[3][5];
        ring_read<P_G>(rg, wf[0]); ring_read<P_G + 1>(rg, wf[1]);
#define G_STEP(i)                                                                                     \
  {                                                                                                   \
    if ((i) == 7 || (i) == 15 || (i) == 28 || (i) == 36) BAR_L();                                     \
    if ((i) % 4 == 0) {          \
      wait_tie4<((i) + 15 < 47 ? 12 : 44 - (i))>(xf[(i) & 15], xf[((i) + 1) & 15], xf[((i) + 2) & 15], xf[((i) + 3) & 15]); \
      xcheck4(xf[(i) & 15], xf[((i) + 1) & 15], xf[((i) + 2) & 15], xf[((i) + 3) & 15], v16, ws, xo + (i) * 4096u); \
    }                                                                                                 \
    kmma<P_G + (i) + 2, ((i) + 2 < 48)>(rg, acc, wf[(i) % 3], xf[(i) & 15], wf[((i) + 2) % 3]);       \
    if ((i) + 16 < 48) GX((i) + 16 < 48 ? (i) + 16 : 0)                                               \
  }
        G_STEP(0) G_STEP(1) FST(35) G_STEP(2) G_STEP(3) G_STEP(4) G_STEP(5) G_STEP(6) G_STEP(7) FST(36)
        G_STEP(8) G_STEP(9) G_STEP(10) G_STEP(11) G_STEP(12) G_STEP(13) G_STEP(14) G_STEP(15)
        G_STEP(16) G_STEP(17) G_STEP(18) G_STEP(19) G_STEP(20) G_STEP(21) G_STEP(22) G_STEP(23) FST(37)
        G_STEP(24) G_STEP(25) G_STEP(26) G_STEP(27) G_STEP(28) G_STEP(29) G_STEP(30) G_STEP(31)
        G_STEP(32) G_STEP(33) G_STEP(34) G_STEP(35) G_STEP(36) G_STEP(37) G_STEP(38) G_STEP(39) FST(38)
        G_STEP(40) G_STEP(41) G_STEP(42) G_STEP(43) G_STEP(44) G_STEP(45) G_STEP(46) G_STEP(47)
        acc_fence(acc);
#undef G_STEP
#undef GX
      } else {
        hstream(k, ws, wt + WT_4 + 85 * 1024, 115, 40, v16);
        WAIT_VM(12);
        BAR();
        hstream(k, ws, wt + WT_4 + 155 * 1024, P_G * 5, 40, v16);
        WAIT_VM(12);
        BAR();
        hstream(k, ws, wt + WT_4 + 195 * 1024, (P_G + 8) * 5, 45, v16);
        WAIT_VM(16);
        BAR();
        if (ly == 0) { poison_tile(k, ws, WS_ACT + ACT_L + A_O, -1, w - 4, lane); poison_hs(k, ws, WS_ACT + ACT_L, w - 4, lane); }
        WAIT_VM(0);
        BAR();
        undef5(acc);
      }
      stamp(k, sb + 18);
      if (w < 4) {
        v2u pk[5];
#pragma unroll
        for (int j = 0; j < 5; ++j) { pk[j] = pk4(x2r[j] + rnd4(x1r[j] + rnd4(acc[j]))); xres[j] = unpk(pk[j]); }
        if (ly == 0) {
          kstore(k, ws, WS_ACT + A_XO, pk, lane);
          kstats(k, ws, 2, xres, lane);
        } else {
          unsigned char* po = opqp(out) + (size_t)(r * 64 + w * 16 + t) * 1280 + (80 * c + 4 * g) * 2;
#pragma unroll
          for (int j = 0; j < 5; ++j) *(GAS v2u*)(po + 32 * j) = pk[j];
        }
      } else {
        undef5(xres);
      }
      stamp(k, sb + 19);
    }
    if (STOP_PHASE == layer * 7 + 5) return;
  }
  stampc(k, 127); stamp(k, 125); stamp_dump(k);
  if (k.b == 0 && tid == 0) __hip_atomic_store((GAS u64*)(wsp + WS_EPOCH), k.tag + 64, __ATOMIC_RELAXED, __HIP_MEMORY_SCOPE_AGENT);
}

extern "C" void kernel_launch(void* const* d_in, const int* in_sizes, int n_in, void* d_out, int out_size, void* d_ws, size_t ws_size,
                              hipStream_t stream) {
  (void)in_sizes; (void)n_in; (void)out_size; (void)ws_size;
  (void)hipFuncSetAttribute((const void*)hedgehog_smlp_kernel, hipFuncAttributeMaxDynamicSharedMemorySize, LDS_BYTES);
  hedgehog_smlp_kernel<<<256, 512, LDS_BYTES, stream>>>(
      (const unsigned char*)d_in[0], (const unsigned char*)d_in[1], (const unsigned char*)d_in[2], (const unsigned char*)d_in[3],
      (const unsigned char*)d_in[5], (const unsigned char*)d_in[6], (const unsigned char*)d_in[7], (const unsigned char*)d_in[8],
      (unsigned char*)d_out, (unsigned char*)d_ws);
}
```
